```python
import torch

def _softmax(x, axis=-1):
    return torch.softmax(x, dim=axis)
def _gelu(x):
    return torch.nn.functional.gelu(x, approximate="tanh")
def _silu(x):
    return torch.nn.functional.silu(x)
def _sigmoid(x):
    return torch.sigmoid(x)
def _layernorm(x, axis=-1, eps=1e-5):
    m = x.mean(dim=axis, keepdim=True)
    v = x.var(dim=axis, keepdim=True, unbiased=False)
    return (x - m) / torch.sqrt(v + eps)
def _scatter_add(tgt, idx, src, axis=0):
    return tgt.clone().index_add_(axis, idx, src)
def _segment_sum(src, seg, n):
    out = torch.zeros((n,) + src.shape[1:], dtype=src.dtype)
    return out.index_add_(0, seg, src)
def _pad(x, pw):
    flat = tuple(v for pair in reversed(pw) for v in pair)
    return torch.nn.functional.pad(x, flat)

kernel_name = 'gated_mix__linear_attn_block__glu_gated_glu__prompt'

def setup_inputs():

    _w = (lambda a: (lambda t: t.to(torch.bfloat16) if t.is_floating_point() else t)(torch.as_tensor(a)))
    def _A_setup():
        B, S, D = 1, 2048, 640
        return {
            "x": _w((lambda *s: torch.randn(*s, dtype=torch.bfloat16) * 0.05)(B, S, D)),
            "wqkv": _w((lambda *s: torch.randn(*s, dtype=torch.bfloat16) * 0.8)(D, 3 * D)),
            "wo": _w((lambda *s: torch.randn(*s, dtype=torch.bfloat16) * 0.2)(D, D)),
        }
    _A_in = _A_setup()

    def _B_setup():
        B, S, D, F = 1, 2048, 640, 1728
        return {
            "x": _w((lambda *s: torch.randn(*s, dtype=torch.bfloat16) * 0.05)(B, S, D)),
            "w_gate": _w((lambda *s: torch.randn(*s, dtype=torch.bfloat16) * 0.4)(D, F)),
            "w_up": _w((lambda *s: torch.randn(*s, dtype=torch.bfloat16) * 0.05)(D, F)),
            "w_down": _w((lambda *s: torch.randn(*s, dtype=torch.bfloat16) * 0.05)(F, D)),
        }
    _B_in = _B_setup()

    return {
        "A_x": _A_in["x"],
        "A_wqkv": _A_in["wqkv"],
        "A_wo": _A_in["wo"],
        "B_x": _B_in["x"],
        "B_w_gate": _B_in["w_gate"],
        "B_w_up": _B_in["w_up"],
        "B_w_down": _B_in["w_down"],
        "gate_w": _w((lambda *s: torch.randn(*s, dtype=torch.bfloat16) * 0.05)(640) * 16.0),
    }

def reference(A_x, A_wqkv, A_wo, B_x, B_w_gate, B_w_up, B_w_down, gate_w):
    def _A_ref(x, wqkv, wo):
        B, S, D = x.shape
        H = 32; Dh = D // H
        qkv = (lambda x, *d: x.permute(*d))((x @ wqkv).reshape(B, S, 3, H, Dh), 2, 0, 3, 1, 4)
        q, k, v = qkv[0], qkv[1], qkv[2]
        phi = lambda t: torch.where(t > 0, t + 1, torch.exp(t)) + 1e-6
        pq, pk = phi(q), phi(k)
        kv = torch.einsum("bhsd,bhse->bhde", pk, v)
        z = torch.einsum("bhsd->bhd", pk)
        num = torch.einsum("bhqd,bhde->bhqe", pq, kv)
        den = torch.einsum("bhqd,bhd->bhq", pq, z)[..., None] + 1e-6
        o = num / den
        return (lambda x, *d: x.permute(*d))(o, 0, 2, 1, 3).reshape(B, S, D) @ wo

    def _B_ref(x, w_gate, w_up, w_down):
        g = torch.einsum("bsd,df->bsf", x, w_gate)
        u = torch.einsum("bsd,df->bsf", x, w_up)
        h = _sigmoid(g) * u
        return torch.einsum("bsf,fd->bsd", h, w_down)

    x = A_x
    g = _sigmoid((x * gate_w).sum(-1, keepdims=True))
    a = _A_ref(x, A_wqkv, A_wo)
    b = _B_ref(x, B_w_gate, B_w_up, B_w_down)
    return g * a + (1 - g) * b
```

```cpp
#include <hip/hip_runtime.h>
#include <stdint.h>

typedef short bf16x8 __attribute__((ext_vector_type(8)));
typedef short s16x4 __attribute__((ext_vector_type(4)));
typedef float f32x4 __attribute__((ext_vector_type(4)));
typedef float f32x2 __attribute__((ext_vector_type(2)));
typedef __bf16 bf16x2_t __attribute__((ext_vector_type(2)));
typedef unsigned u32x4 __attribute__((ext_vector_type(4)));
typedef unsigned u32x2 __attribute__((ext_vector_type(2)));
#define LAS __attribute__((address_space(3)))
#define DI __device__ __forceinline__
typedef __amdgpu_buffer_rsrc_t rsrc_t;

constexpr int S_ = 2048, D_ = 640, F_ = 1728;
constexpr int KSH = 54, KS = 74;
constexpr int SC1 = 16;

constexpr unsigned WS_E = 0;
constexpr unsigned FS = 128;
constexpr unsigned WS_F1 = 1024;
constexpr unsigned WS_FM = WS_F1 + 256 * FS;
constexpr unsigned WS_FG = WS_FM + 16 * FS;
constexpr unsigned WS_A = 131072;
constexpr unsigned WS_A_BYTES = 32u * KS * 4096u;
constexpr unsigned WS_B = WS_A + WS_A_BYTES;
constexpr unsigned WS_B_BYTES = 8u * KS * 5120u;
constexpr unsigned WS_KVP = WS_B + WS_B_BYTES;
constexpr unsigned WS_KVP_BYTES = 8u * 32u * 420u * 4u;
constexpr unsigned WS_G = WS_KVP + WS_KVP_BYTES;
constexpr unsigned WS_END = WS_G + 2048u * 4u;

DI rsrc_t mkrsrc(const void* p, unsigned bytes) { return __builtin_amdgcn_make_buffer_rsrc((void*)p, (short)0, (int)bytes, 0x00020000); }
DI unsigned rfl(unsigned v) { return __builtin_amdgcn_readfirstlane(v); }
template <int N> DI unsigned wrap(unsigned t) { unsigned r; asm("s_sub_u32 %0, %1, %2\n\ts_min_u32 %0, %0, %1" : "=&s"(r) : "s"(t), "i"(N) : "scc"); return r; }
constexpr unsigned OOB = 0x80000000u;
DI unsigned sel(bool c, unsigned a, unsigned b) { return c ? a : b; }
DI unsigned opq(unsigned v) { asm volatile("" : "+s"(v)); return v; }
DI unsigned pk2(float a, float b) { f32x2 v = {a, b}; return __builtin_bit_cast(unsigned, __builtin_convertvector(v, bf16x2_t)); }
DI float bflo(unsigned u) { return __builtin_bit_cast(float, u << 16); }
DI float bfhi(unsigned u) { return __builtin_bit_cast(float, u & 0xffff0000u); }
DI float dot2(unsigned a, unsigned b, float c) { return __builtin_amdgcn_fdot2_f32_bf16(__builtin_bit_cast(bf16x2_t, a), __builtin_bit_cast(bf16x2_t, b), c, false); }
DI f32x4 mfma16(bf16x8 a, bf16x8 b, f32x4 c) { return __builtin_amdgcn_mfma_f32_16x16x32_bf16(a, b, c, 0, 0, 0); }
DI float sigmoidf_(float v) { return __builtin_amdgcn_rcpf(1.0f + __expf(-v)); }
constexpr float PHI_EPS = 1e-6f, LOG2E = 1.4426950408889634f;
DI f32x4 phi4(f32x4 u) {
  const f32x4 a = u * LOG2E - PHI_EPS * LOG2E;
  f32x4 e, m;
#pragma unroll
  for (int r = 0; r < 4; ++r) { e[r] = __builtin_amdgcn_fmed3f(__builtin_amdgcn_exp2f(a[r]), 0.f, 1.f); m[r] = __builtin_amdgcn_fmed3f(u[r], PHI_EPS, 3.0e38f); }
  return e + m;
}
DI f32x4 glu4(f32x4 g, f32x4 u) {
  f32x4 e = g * -LOG2E;
#pragma unroll
  for (int r = 0; r < 4; ++r) e[r] = __builtin_amdgcn_exp2f(e[r]);
  e = e + 1.0f;
#pragma unroll
  for (int r = 0; r < 4; ++r) e[r] = __builtin_amdgcn_rcpf(e[r]);
  return e * u;
}
#define BF(v) __builtin_bit_cast(bf16x8, v)
#define F4(v) __builtin_bit_cast(f32x4, v)
#define U4(v) __builtin_bit_cast(u32x4, v)
DI u32x4 ld128(rsrc_t rs, unsigned v, unsigned s = 0) { return __builtin_amdgcn_raw_buffer_load_b128(rs, v, s, SC1); }
DI void st128(u32x4 d, rsrc_t rs, unsigned v) { __builtin_amdgcn_raw_buffer_store_b128(d, rs, v, 0, SC1); }
DI void st64(u32x2 d, rsrc_t rs, unsigned v) { __builtin_amdgcn_raw_buffer_store_b64(d, rs, v, 0, SC1); }

DI void dma16(rsrc_t rs, unsigned voff, unsigned soff, unsigned ldsb) {
  asm volatile("s_mov_b32 m0, %0\n\ts_nop 0\n\tbuffer_load_dwordx4 %1, %2, %3 offen lds" ::"s"(ldsb), "v"(voff), "s"(rs), "s"(soff) : "memory");
}
DI void dma16_sc1(rsrc_t rs, unsigned voff, unsigned soff, unsigned ldsb) {
  asm volatile("s_mov_b32 m0, %0\n\ts_nop 0\n\tbuffer_load_dwordx4 %1, %2, %3 offen sc1 lds" ::"s"(ldsb), "v"(voff), "s"(rs), "s"(soff) : "memory");
}
DI void dma16_nt(rsrc_t rs, unsigned voff, unsigned soff, unsigned ldsb) {
  asm volatile("s_mov_b32 m0, %0\n\ts_nop 0\n\tbuffer_load_dwordx4 %1, %2, %3 offen nt lds" ::"s"(ldsb), "v"(voff), "s"(rs), "s"(soff) : "memory");
}
DI void dma16_sc1_if(unsigned on, rsrc_t rs, unsigned voff, unsigned soff, unsigned ldsb) {
  asm volatile("s_cmp_eq_u32 %4, 0\n\ts_cbranch_scc1 1f\n\ts_mov_b32 m0, %0\n\ts_nop 0\n\tbuffer_load_dwordx4 %1, %2, %3 offen sc1 lds\n1:"
               ::"s"(ldsb), "v"(voff), "s"(rs), "s"(soff), "s"(on) : "memory", "scc");
}
DI void dma4_if(unsigned on, rsrc_t rs, unsigned voff, unsigned soff, unsigned ldsb) {
  asm volatile("s_cmp_eq_u32 %4, 0\n\ts_cbranch_scc1 1f\n\ts_mov_b32 m0, %0\n\ts_nop 0\n\tbuffer_load_dword %1, %2, %3 offen lds\n1:"
               ::"s"(ldsb), "v"(voff), "s"(rs), "s"(soff), "s"(on) : "memory", "scc");
}
#define WAIT_BAR(N, N1, extra) asm volatile("s_cmp_lg_u32 %0, 0\n\ts_cbranch_scc1 1f\n\ts_waitcnt vmcnt(" #N ")\n1:\n\ts_waitcnt vmcnt(" #N1 ") lgkmcnt(0)\n\ts_barrier" ::"s"(extra) : "memory", "scc")
DI bf16x8 lds128(unsigned a) { return *(const LAS bf16x8*)(a); }
DI s16x4 ldstr(unsigned a) { return __builtin_amdgcn_ds_read_tr16_b64_v4i16((LAS s16x4*)(a)); }
#define SBAR() __builtin_amdgcn_sched_barrier(0)
#define MEMFENCE() asm volatile("" ::: "memory")
#define DRAIN_BAR() asm volatile("s_waitcnt vmcnt(0) lgkmcnt(0)\n\ts_barrier" ::: "memory")
#define LDS_BAR() asm volatile("s_waitcnt lgkmcnt(0)\n\ts_barrier" ::: "memory")
#define STAMP_DECL
#define STAMP(i)
#define STAMP_FLUSH(k)
#define STAMP_ARG
#define STAMP_PASS

template <int N> DI unsigned flag_addr(unsigned lane, unsigned off) { return sel(lane < N, off, OOB); }
DI u32x2 flag_load(rsrc_t rs, unsigned voff) { return __builtin_amdgcn_raw_buffer_load_b64(rs, voff, 0, SC1); }
template <int N, int SLEEP = 2> DI void flag_wait(u32x2 v, rsrc_t rs, unsigned voff, unsigned long long tag) {
  unsigned n = 1u << 16;
  const unsigned long long mask = N == 64 ? ~0ull : (1ull << (N & 63)) - 1;
  asm volatile("1:\n\tv_cmp_ne_u64 vcc, %3, %0\n\ts_and_b64 vcc, vcc, %5\n\ts_cbranch_scc0 2f\n\ts_sub_u32 %1, %1, 1\n\ts_cbranch_scc1 2f\n\ts_sleep %6\n\t"
               "buffer_load_dwordx2 %0, %2, %4, 0 offen sc1\n\ts_waitcnt vmcnt(0)\n\ts_branch 1b\n2:"
               : "+v"(v), "+s"(n) : "v"(voff), "s"(tag), "s"(rs), "s"(mask), "i"(SLEEP) : "memory", "scc", "vcc");
}
DI void flag_set(rsrc_t rs, unsigned off, unsigned long long tag) { st64(u32x2{(unsigned)tag, (unsigned)(tag >> 32)}, rs, sel(threadIdx.x == 0, off, OOB)); }

constexpr unsigned BUFB = 57344, L_W = 32768, WIMG = 12288;
constexpr unsigned L_SCR = 30 * 5120, LDS_BYTES = 163840;
DI unsigned wave_slots(unsigned w) { return w < 5 ? 0x0c0b0100u + w * 0x02020202u : (w == 5 ? 0x1615170au : (w == 6 ? 0x1e1a1918u : 0x1f1d1c1bu)); }
DI unsigned slot_addr(unsigned slots, int k) { return ((slots >> (8 * k)) & 0xff) * 5120; }

template <int NB> struct Frags { bf16x8 x[4]; s16x4 wl[NB], wh[NB]; };
struct Dma { rsrc_t rx, rw, rp; unsigned vx[2], vw[3], sx, ldw, mx, mw, vp, pstep, mp, pf; };
struct Ctx { rsrc_t rws; unsigned long long tag; unsigned lane, vb, last2, esrc[3], edst[3]; };

DI void dma_piece(const Dma& d, int i, unsigned t, unsigned buf) {
  if (i < 4) dma16(d.rx, d.vx[i & 1], d.sx + i * (8 * 1280) + t * 128, d.mx + buf * BUFB + i * 1024);
  else dma16(d.rw, d.vw[i - 4], t * 64 * d.ldw, d.mw + buf * BUFB + (i - 4) * 1024);
}
DI void dma_early(const Ctx& c, int i) {
  if (i < 10) dma16_sc1(c.rws, c.vb, c.esrc[i / 5] + (i % 5) * 1024, c.edst[i / 5] + (i % 5) * 1024);
  else dma16_sc1_if(c.last2, c.rws, c.vb, c.esrc[2] + (i % 5) * 1024, c.edst[2] + (i % 5) * 1024);
}
template <int NB>
DI void read_piece(Frags<NB>& f, int r, unsigned xa, const unsigned (&wa)[NB], unsigned xoff, unsigned woff) {
  if (r == 0) f.x[0] = lds128(xa + xoff);
  else if (r <= 2 * NB) {
    const int b = (r - 1) >> 1;
    if ((r - 1) & 1) f.wh[b] = ldstr(wa[b] + woff + 768); else f.wl[b] = ldstr(wa[b] + woff);
  } else if (r < 2 * NB + 4) f.x[r - 2 * NB] = lds128(xa + xoff + (r - 2 * NB) * 2048);
}
template <int NB, bool SWAP, bool RD, bool DM>
DI void half_step(f32x4 (&acc)[4][NB], const Frags<NB>& cur, Frags<NB>& nxt, unsigned xa, const unsigned (&wa)[NB],
                  unsigned xoff, unsigned woff, const Dma& d, unsigned t, unsigned buf) {
#pragma unroll
  for (int g = 0; g < 8; ++g) {
    const int i0 = (g * 4 * NB) / 8, i1 = ((g + 1) * 4 * NB) / 8;
#pragma unroll
    for (int i = i0; i < i1; ++i) {
      const int m = i / NB, b = i % NB;
      const bf16x8 wf = __builtin_shufflevector(cur.wl[b], cur.wh[b], 0, 1, 2, 3, 4, 5, 6, 7);
      acc[m][b] = SWAP ? mfma16(wf, cur.x[m], acc[m][b]) : mfma16(cur.x[m], wf, acc[m][b]);
      if (i == i0) {
        if (RD) {
#pragma unroll
          for (int r = 3 * g; r < 3 * g + 3; ++r) read_piece<NB>(nxt, r, xa, wa, xoff, woff);
        }
        SBAR();
      }
      if (DM && i == i0 + 1 && g < 7) { dma_piece(d, g, t, buf); SBAR(); }
    }
    SBAR();
  }
}

template <int NB, bool SWAP>
DI void mma_row(f32x4 (&acc)[4][NB], const Frags<NB>& f, int m) {
#pragma unroll
  for (int b = 0; b < NB; ++b) {
    const bf16x8 wf = __builtin_shufflevector(f.wl[b], f.wh[b], 0, 1, 2, 3, 4, 5, 6, 7);
    acc[m][b] = SWAP ? mfma16(wf, f.x[m], acc[m][b]) : mfma16(f.x[m], wf, acc[m][b]);
  }
}
template <int NMFMA, int NVALU> DI void interleave() {
#pragma unroll
  for (int i = 0; i < NMFMA; ++i) { __builtin_amdgcn_sched_group_barrier(0x8, 1, 0); __builtin_amdgcn_sched_group_barrier(0x402, NVALU, 0); }
}

template <int NB, bool SWAP, int NV, bool EARLY, class Epi>
DI void gemm1_loop(f32x4 (&acc)[4][NB], const Dma& d, const Ctx& c, unsigned xa0, unsigned xa1, const unsigned (&wa)[NB], Epi epi STAMP_ARG) {
  Frags<NB> f0, f1;
  STAMP(1);
  WAIT_BAR(7, 8, d.pf);
  STAMP(2);
  dma4_if(d.pf, d.rp, d.vp, 3 * d.pstep, d.mp);
#pragma unroll
  for (int r = 0; r < 2 * NB + 4; ++r) read_piece<NB>(f0, r, xa0, wa, 0, 0);
  SBAR();
#pragma unroll 1
  for (unsigned t = 0; t < 8; t += 2) {
    const unsigned pf = t < 6 ? d.pf : 0;
    half_step<NB, SWAP, true, false>(acc, f0, f1, xa1, wa, 0, 32 * 192, d, 0, 0);
    WAIT_BAR(0, 1, d.pf); SBAR();
    half_step<NB, SWAP, true, true>(acc, f1, f0, xa0, wa, BUFB, BUFB, d, t + 2, 0);
    dma4_if(pf, d.rp, d.vp, (t + 4) * d.pstep, d.mp); SBAR();
    half_step<NB, SWAP, true, false>(acc, f0, f1, xa1, wa, BUFB, BUFB + 32 * 192, d, 0, 0);
    WAIT_BAR(0, 1, pf); SBAR();
    half_step<NB, SWAP, true, true>(acc, f1, f0, xa0, wa, 0, 0, d, t + 3, 1);
    dma4_if(pf, d.rp, d.vp, (t + 5) * d.pstep, d.mp); SBAR();
  }
  const unsigned fsp = flag_addr<16>(c.lane, WS_F1 + ((c.lane & 7) * 32 + 23 + (c.lane & 8)) * FS);
  const u32x2 fv = flag_load(c.rws, fsp);
  SBAR();
  half_step<NB, SWAP, true, false>(acc, f0, f1, xa1, wa, 0, 32 * 192, d, 0, 0);
  DRAIN_BAR(); SBAR();
  STAMP(3);
  flag_wait<16>(fv, c.rws, fsp, c.tag);
  SBAR();
  Frags<NB>& fa = f0;
  Frags<NB> fb;
#pragma unroll
  for (int i = 0; i < 4 * NB; ++i) {
    const int b = i % NB, st = i / NB;
    const Frags<NB>& f = st < 2 ? f1 : (st == 2 ? fa : fb);
    const int m = st == 1 ? 1 : 0;
    const bf16x8 wf = __builtin_shufflevector(f.wl[b], f.wh[b], 0, 1, 2, 3, 4, 5, 6, 7);
    acc[m][b] = SWAP ? mfma16(wf, f.x[m], acc[m][b]) : mfma16(f.x[m], wf, acc[m][b]);
    constexpr int NR = 2 * NB + 1;
#pragma unroll
    for (int r = 3 * i; r < 3 * i + 3; ++r) {
      if (r < NR) read_piece<NB>(fa, r, xa0, wa, BUFB, BUFB);
      else if (r < 2 * NR) read_piece<NB>(fb, r - NR, xa1, wa, BUFB, BUFB + 32 * 192);
      else if (r < 2 * NR + 6) {
        if ((r - 2 * NR) & 1) read_piece<NB>(fb, NR + (r - 2 * NR) / 2, xa1, wa, BUFB, 0);
        else read_piece<NB>(fa, NR + (r - 2 * NR) / 2, xa0, wa, BUFB, 0);
      }
    }
    SBAR();
    if (EARLY && i % 4 == 3 && i / 4 < 5) { dma_early(c, i / 4); SBAR(); }
  }
#pragma unroll
  for (int m = 1; m < 4; ++m) {
#pragma unroll
    for (int i = 0; i < 4; ++i) if (EARLY && 4 * m + 1 + i < 15) dma_early(c, 4 * m + 1 + i);
    SBAR();
    mma_row<NB, SWAP>(acc, fa, m); mma_row<NB, SWAP>(acc, fb, m);
    if (m < 3) mma_row<NB, SWAP>(acc, f1, m + 1);
    epi(m - 1);
    interleave<3 * NB, NV>();
    SBAR();
  }
  STAMP(4);
  epi(3);
}

DI u32x4 pair_tail(u32x2 a, u32x2 b) {
  const auto r0 = __builtin_amdgcn_permlane32_swap(a[0], b[0], false, false);
  const auto r1 = __builtin_amdgcn_permlane32_swap(a[1], b[1], false, false);
  return u32x4{r0[0], r1[0], r0[1], r1[1]};
}

__global__ void __launch_bounds__(512, 2)
fused_block(const uint16_t* __restrict__ x, const uint16_t* __restrict__ wqkv, const uint16_t* __restrict__ wo,
            const uint16_t* __restrict__ wg, const uint16_t* __restrict__ wu, const uint16_t* __restrict__ wd,
            const uint16_t* __restrict__ gw, unsigned char* __restrict__ ws, uint16_t* __restrict__ out) {
  extern __shared__ __attribute__((aligned(16))) unsigned char smem[];
  STAMP_DECL STAMP(0);
  const unsigned lds0 = (unsigned)(uintptr_t)smem;
  const unsigned tid = threadIdx.x, lane = tid & 63, w = rfl(tid >> 6);
  const unsigned bid = blockIdx.x, xcd = bid & 7, idx = bid >> 3;
  const unsigned rt = 4 * (xcd & 1) + (idx & 3), slot = idx >> 2, j = xcd >> 1;
  const rsrc_t rws = mkrsrc(ws, WS_END);
  const unsigned G = lane >> 4, l15 = lane & 15;
  const unsigned myflag = (rt * 32 + 8 * j + slot) * FS;
  const unsigned pan = 4 * (xcd & 1) + j, sub = idx & 3, ct = slot, rt64p = 4 * pan + sub;
  const unsigned hs0 = wrap<54>(wrap<54>(7 * w - (w == 7) + 7 * ct + 2 * sub));
  auto hstep = [&](int i) -> unsigned { return wrap<54>(hs0 + i); };
  const unsigned slots = wave_slots(w);
  Ctx c;
  c.rws = rws; c.lane = lane;
  c.vb = WS_B + ct * KS * 5120 + lane * 16;
  c.last2 = (0xc0u >> w) & 1;
#pragma unroll
  for (int k = 0; k < 3; ++k) { c.esrc[k] = hstep(3 + k) * 5120; c.edst[k] = lds0 + slot_addr(slots, k); }
  unsigned long long epoch = *(const __attribute__((address_space(4))) unsigned long long*)(ws + WS_E);
#define TAKE_TAG() do { asm volatile("" : "+s"(epoch)); c.tag = epoch + 1; } while (0)
  const unsigned fg = WS_FG + (2 * pan + (sub >> 1)) * FS;
  const unsigned gt0 = hs0 / 3 + (lane & 3), gt = gt0 - sel(gt0 >= 18, 18, 0);
  const unsigned gcs = gt + 3 + sel(gt >= 5, 3, 0) + sel(gt >= 10, 3, 0) + sel(gt >= 14, 4, 0);
  const unsigned fh = sel(lane < 3, WS_F1 + (pan * 32 + gcs) * FS, sel(lane == 3, fg, OOB));
  auto preload_b = [&](u32x4 (&bq)[3][5], u32x2& fhv) __attribute__((always_inline)) {
#pragma unroll
    for (int b = 0; b < 5; ++b) bq[0][b] = ld128(rws, c.vb + b * 1024, hstep(0) * 5120);
#pragma unroll
    for (int b = 0; b < 10; ++b) dma16_sc1_if(c.last2 ^ 1, rws, c.vb, hstep(5 + b / 5) * 5120 + (b % 5) * 1024, lds0 + slot_addr(slots, 2 + b / 5) + (b % 5) * 1024);
    SBAR();
    fhv = flag_load(rws, fh);
    SBAR();
#pragma unroll
    for (int i = 1; i < 3; ++i)
#pragma unroll
      for (int b = 0; b < 5; ++b) bq[i][b] = ld128(rws, c.vb + b * 1024, hstep(i) * 5120);
    SBAR();
  };
  auto phase2 = [&](u32x4 (&bq)[3][5], u32x2 fhv) __attribute__((always_inline)) {
    STAMP(8);
    f32x4 acc[4][5];
#pragma unroll
    for (int i = 0; i < 20; ++i) acc[i / 5][i % 5] = f32x4{0.f, 0.f, 0.f, 0.f};
    const unsigned va = WS_A + rt64p * KS * 4096 + lane * 16;
    u32x4 aq[5][4];
    auto mma = [&](int ia, int ib) __attribute__((always_inline)) {
#pragma unroll
      for (int m = 0; m < 4; ++m)
#pragma unroll
        for (int b = 0; b < 5; ++b) acc[m][b] = mfma16(BF(bq[ib][b]), BF(aq[ia][m]), acc[m][b]);
      SBAR();
    };
    flag_wait<4>(fhv, rws, fh, c.tag);
    STAMP(9);
#pragma unroll
    for (int i = 0; i < 5; ++i)
#pragma unroll
      for (int m = 0; m < 4; ++m) aq[i][m] = ld128(rws, va + m * 1024, hstep(i) * 4096);
    float gm[4];
#pragma unroll
    for (int m = 0; m < 4; ++m) gm[m] = __builtin_bit_cast(float, __builtin_amdgcn_raw_buffer_load_b32(rws, WS_G + (rt64p * 64 + m * 16 + l15) * 4, 0, SC1));
    SBAR();
    const unsigned ps0 = wrap<20>(wrap<20>((w < 4 ? 2 * w : 3 * w - 4) + ((5 * ct) >> 1) + 5 * sub));
    auto pstep = [&](int i) -> unsigned { return KSH + wrap<20>(ps0 + i); };
    const unsigned pq0 = ps0 / 5;
    const unsigned fp = sel(lane < 8, WS_FM + ((4 * pq0 + lane) & 15) * FS, sel(lane < 10, WS_F1 + (pan * 32 + 8 * ((pq0 + lane) & 3)) * FS, OOB));
    u32x2 fpv;
#pragma unroll
    for (int i = 0; i < 6; ++i) {
      mma(i % 5, i % 3);
      STAMP(10 + i);
      if (i < 2) {
        const unsigned vz = va | (i == 1 && c.last2 ? OOB : 0u);
#pragma unroll
        for (int m = 0; m < 4; ++m) aq[i][m] = ld128(rws, vz + m * 1024, hstep(i + 5) * 4096);
      }
      if (i < 4) {
        const unsigned sa = lds0 + slot_addr(slots, i) + lane * 16;
#pragma unroll
        for (int b = 0; b < 5; ++b) bq[i % 3][b] = U4(lds128(sa + b * 1024));
      }
      if (i == 3) fpv = flag_load(rws, fp);
      SBAR();
    }
    flag_wait<10>(fpv, rws, fp, c.tag);
    STAMP(16);
    const unsigned third = w < 4 ? OOB : 0u;
#pragma unroll
    for (int i = 0; i < 3; ++i) {
#pragma unroll
      for (int m = 0; m < 4; ++m) aq[2 + i][m] = ld128(rws, (va | (i == 2 ? third : 0u)) + m * 1024, pstep(i) * 4096);
      if (i < 2) {
#pragma unroll
        for (int b = 0; b < 5; ++b) bq[1 + i][b] = ld128(rws, c.vb + b * 1024, pstep(i) * 5120);
      }
    }
    SBAR();
    if (opq(w) < 6) mma(1, 0);
#pragma unroll
    for (int b = 0; b < 5; ++b) bq[0][b] = ld128(rws, (c.vb | third) + b * 1024, pstep(2) * 5120);
    SBAR();
#pragma unroll
    for (int i = 0; i < 20; ++i) acc[i / 5][i % 5] *= 1.0f - gm[i / 5];
    SBAR();
    STAMP(17);
    mma(2, 1);
    STAMP(18);
    mma(3, 2);
    if (opq(w) >= 4) mma(4, 0);
    STAMP(19);
    u32x2 fall;
    if (bid == 0) fall = flag_load(rws, sel(tid < 256, WS_F1 + tid * FS, WS_F1));
#pragma unroll
    for (int m = 0; m < 4; ++m) {
      LAS f32x4* p = (LAS f32x4*)(lds0 + slot_addr(slots, m)) + lane;
#pragma unroll
      for (int b = 0; b < 5; ++b) p[b * 64] = acc[m][b];
    }
    STAMP(20);
    LDS_BAR();
    STAMP(21);
    const rsrc_t ro = mkrsrc(out, S_ * D_ * 2);
#pragma unroll
    for (int r = 0; r < 3; ++r) {
      const unsigned f = w + 8 * r;
      if (f < 20) {
        const unsigned m = f / 5, b = f % 5;
        f32x4 p[8];
#pragma unroll
        for (int ww = 0; ww < 8; ++ww) p[ww] = *(const LAS f32x4*)(lds0 + ((wave_slots(ww) >> (8 * m)) & 0xff) * 5120 + b * 1024 + lane * 16);
        const f32x4 v = ((p[0] + p[1]) + (p[2] + p[3])) + ((p[4] + p[5]) + (p[6] + p[7]));
        __builtin_amdgcn_raw_buffer_store_b64(u32x2{pk2(v[0], v[1]), pk2(v[2], v[3])}, ro, (rt64p * 64 + m * 16 + l15) * 1280 + (ct * 80 + b * 16 + 4 * G) * 2, 0, 0);
      }
    }
    if (bid == 0) {
      flag_wait<64>(fall, rws, sel(tid < 256, WS_F1 + tid * FS, WS_F1), c.tag);
      __builtin_amdgcn_s_barrier();
      flag_set(rws, WS_E, c.tag);
    }
    STAMP(22);
    STAMP_FLUSH(0);
  };
  u32x4 bq[3][5];
  u32x2 fhv;

  const unsigned type = (j >= 2 && slot == 7) ? 3 : (slot == 0 ? 0 : (slot < 3 ? 1 : 2));
  if (opq(type) == 3) {
    const unsigned sp = (j - 2) * 8 + rt;
    const rsrc_t rwd = mkrsrc(wd, F_ * D_ * 2);
    unsigned vo[5];
#pragma unroll
    for (int i = 0; i < 5; ++i) {
      const unsigned q = 64 * i + lane, r = q / 80, pc = q % 80;
      vo[i] = r * 1280 + ((pc ^ (((4 * w + r) & 7) << 1)) << 4);
    }
    asm volatile("s_nop 4" ::: "memory");
#pragma unroll
    for (int it = 0; it < 4; ++it) {
      const unsigned s = sp + 16 * it, cc = s / 3, u = s % 3;
      const unsigned f0 = 96 * cc + (u == 1 ? 48 * ((w >> 1) & 1) + 32 + 4 * ((w & 1) + 2 * (w >> 2)) : (u == 2 ? 48 : 0) + 4 * w);
#pragma unroll
      for (int i = 0; i < 5; ++i) dma16_nt(rwd, vo[i], f0 * 1280, lds0 + it * 40960 + w * 5120 + i * 1024);
    }
    const unsigned q4 = l15 >> 2, p4 = lane & 3;
    TAKE_TAG();
#pragma unroll
    for (int it = 0; it < 4; ++it) {
      asm volatile("s_waitcnt vmcnt(15)\n\ts_barrier" ::: "memory");
      const unsigned s = sp + 16 * it;
      const unsigned dead = s < KSH ? 0u : OOB;
#pragma unroll
      for (int nb = 0; nb < 5; ++nb) {
        const unsigned row = 4 * G + q4, ch = (w * 5 + nb) * 2 + (p4 >> 1);
        const unsigned a = lds0 + it * 40960 + row * 1280 + ((ch ^ ((row & 7) << 1)) << 4) + (p4 & 1) * 8;
        const s16x4 lo = ldstr(a), hi = ldstr(a + 16 * 1280);
        st128(U4(__builtin_shufflevector(lo, hi, 0, 1, 2, 3, 4, 5, 6, 7)), rws, (WS_B + ((w * KS + s) * 5 + nb) * 1024 + lane * 16) | dead);
      }
      SBAR();
    }
    DRAIN_BAR();
    flag_set(rws, WS_F1 + myflag, c.tag);
    STAMP(1);
    const unsigned hA = 4 * (sp >> 1) + (sp & 1);
    {
      const rsrc_t rwo = mkrsrc(wo, D_ * D_ * 2);
#pragma unroll
      for (int i = 0; i < 7; ++i) {
        const unsigned q = min(i * 64 + lane, 399u), r = q / 10;
        dma16(rwo, (20 * hA + r + (r < 20 ? 0 : 20)) * 1280 + 160 * w + (q % 10) * 16, 0, lds0 + BUFB + w * 7168 + i * 1024);
      }
    }
    const rsrc_t rx = mkrsrc(x, S_ * D_ * 2), rg = mkrsrc(gw, D_ * 2);
    const unsigned tok0 = rt * 256 + (j - 2) * 128 + w * 16 + G;
    u32x4 gv[5], xv[4][5];
#pragma unroll
    for (int cc = 0; cc < 5; ++cc) gv[cc] = __builtin_amdgcn_raw_buffer_load_b128(rg, (cc * 16 + l15) * 16, 0, 0);
#pragma unroll
    for (int r = 0; r < 4; ++r)
#pragma unroll
      for (int cc = 0; cc < 5; ++cc) xv[r][cc] = __builtin_amdgcn_raw_buffer_load_b128(rx, (tok0 + r * 4) * 1280 + (cc * 16 + l15) * 16, 0, 0);
    ((LAS u32x2*)(lds0 + L_SCR))[tid] = u32x2{0, 0};
    SBAR();
#pragma unroll
    for (int r = 0; r < 4; ++r) {
      float s = 0.f;
#pragma unroll
      for (int cc = 0; cc < 5; ++cc)
#pragma unroll
        for (int e = 0; e < 4; ++e) s = dot2(xv[r][cc][e], gv[cc][e], s);
      s += __shfl_xor(s, 1); s += __shfl_xor(s, 2); s += __shfl_xor(s, 4); s += __shfl_xor(s, 8);
      __builtin_amdgcn_raw_buffer_store_b32(__builtin_bit_cast(unsigned, sigmoidf_(s)), rws, sel(l15 == 0, WS_G + (tok0 + r * 4) * 4, OOB), 0, SC1);
    }
    asm volatile("s_waitcnt vmcnt(4)" ::: "memory");
    bf16x8 wof[2][5];
#pragma unroll
    for (int i = 0; i < 10; ++i) {
      const unsigned wb = lds0 + BUFB + w * 7168 + (i / 5) * 3200 + (i % 5) * 32 + p4 * 8;
      wof[i / 5][i % 5] = __builtin_shufflevector(ldstr(wb + min(8 * G + q4, 19u) * 160), ldstr(wb + min(8 * G + 4 + q4, 19u) * 160), 0, 1, 2, 3, 4, 5, 6, 7);
    }
    const unsigned fsp = flag_addr<16>(lane, WS_F1 + ((lane & 7) * 32 + 23 + (lane & 8)) * FS);
    const u32x2 fsv = flag_load(rws, fsp);
    DRAIN_BAR();
    flag_set(rws, WS_FG + (2 * rt + j - 2) * FS, c.tag);
    STAMP(2);
    flag_wait<16>(fsv, rws, fsp, c.tag);
#pragma unroll
    for (int i = 0; i < 15; ++i) dma_early(c, i);
    preload_b(bq, fhv);
    STAMP(5);
    if (w < 4) {
      const unsigned fkv = flag_addr<8>(lane, WS_F1 + (lane * 32 + 8 * (sp >> 2) + 1 + ((sp >> 1) & 1)) * FS);
      flag_wait<8>(flag_load(rws, fkv), rws, fkv, c.tag);
      STAMP(6);
      const unsigned t = tid & 127, ko = sel(t < 100, WS_KVP + (hA + 2 * (w >> 1)) * 1680 + t * 16, OOB);
      f32x4 p[8];
#pragma unroll
      for (int r = 0; r < 8; ++r) p[r] = F4(ld128(rws, ko + r * (32 * 1680)));
      const f32x4 sv = ((p[0] + p[1]) + (p[2] + p[3])) + ((p[4] + p[5]) + (p[6] + p[7]));
      const unsigned u0 = pk2(sv[0], sv[1]), u1 = pk2(sv[2], sv[3]);
      LAS unsigned short* ks = (LAS unsigned short*)(lds0 + L_SCR + (w >> 1) * 2048) + (t % 5) * 128 + t / 5;
      ks[0] = (unsigned short)u0; ks[32] = (unsigned short)(u0 >> 16); ks[64] = (unsigned short)u1; ks[96] = (unsigned short)(u1 >> 16);
    }
    LDS_BAR();
    {
      bf16x8 a0[2], a1[2];
#pragma unroll
      for (int k = 0; k < 2; ++k) { a0[k] = lds128(lds0 + L_SCR + k * 2048 + l15 * 64 + G * 16); a1[k] = lds128(lds0 + L_SCR + k * 2048 + (16 + (l15 & 3)) * 64 + G * 16); }
      const unsigned kch = (sp >> 1) & 1;
      const unsigned mo = WS_B + (w * KS + KSH + 5 * (sp >> 2)) * 5120 + l15 * 16;
#pragma unroll
      for (int i = 0; i < 5; ++i) {
        const f32x4 z4 = {0.f, 0.f, 0.f, 0.f};
        const f32x4 dA = mfma16(a0[0], wof[0][i], z4), dB = mfma16(a0[1], wof[1][i], z4), tA = mfma16(a1[0], wof[0][i], z4), tB = mfma16(a1[1], wof[1][i], z4);
        const auto r0 = __builtin_amdgcn_permlane16_swap(pk2(dA[0], dA[1]), pk2(dB[0], dB[1]), false, false);
        const auto r1 = __builtin_amdgcn_permlane16_swap(pk2(dA[2], dA[3]), pk2(dB[2], dB[3]), false, false);
        st128(u32x4{r0[0], r1[0], r0[1], r1[1]}, rws, mo + i * 1024 + (2 * kch + (G >> 1)) * 5120 + ((sp & 1) + 2 * (G & 1)) * 256);
        st128(u32x4{pk2(tA[0], tA[1]), pk2(tA[2], tA[3]), pk2(tB[0], tB[1]), pk2(tB[2], tB[3])}, rws, sel(G == 0, mo + i * 1024 + 4 * 5120 + (2 * kch + (sp & 1)) * 256, OOB));
      }
    }
    STAMP(7);
    DRAIN_BAR();
    flag_set(rws, WS_FM + sp * FS, c.tag);
  }
  if (opq(type) != 3) {
#pragma unroll
    for (int i = 0; i < 3; ++i) asm volatile("" : "=v"(bq[i][0]), "=v"(bq[i][1]), "=v"(bq[i][2]), "=v"(bq[i][3]), "=v"(bq[i][4]));
    const unsigned ci = type == 0 ? j : (type == 1 ? 2 * j + slot - 1 : (j < 2 ? 5 * j + slot - 3 : 10 + 4 * (j - 2) + slot - 3));
    const unsigned half = w >> 2;
    Dma d;
    d.rx = mkrsrc(x, S_ * D_ * 2);
    unsigned col0, shift = 0, cmax;
    if (type == 2) { d.rw = mkrsrc(half ? wu : wg, D_ * F_ * 2); d.ldw = F_ * 2; col0 = 96 * ci; cmax = 11; }
    else {
      d.rw = mkrsrc(wqkv, D_ * 3 * D_ * 2); d.ldw = 3 * D_ * 2; cmax = 9;
      if (type == 0) col0 = 160 * ci + 80 * half; else { col0 = 640 + 640 * half + 80 * ci; shift = half; }
    }
    {
      const bool isx = w < 6;
      const unsigned pimg = w & 1;
      const unsigned pcol = type == 2 ? col0 : (type == 0 ? 160 * ci + 80 * pimg : 640 + 640 * pimg + 80 * ci);
      d.rp = isx ? d.rx : (type == 2 ? mkrsrc(pimg ? wu : wg, D_ * F_ * 2) : d.rw);
      d.pstep = isx ? 128 : 64 * d.ldw;
      d.vp = isx ? (rt * 256 + (slot + 5 - w) * 32 + (lane & 31)) * 1280
                 : (16 * (idx & 3) + ((lane >> 1) & 15)) * d.ldw + pcol * 2 + (lane & 1) * (cmax * 16 + 12);
      d.mp = lds0 + LDS_BYTES - 256;
      d.pf = (((j >= 2 && slot == 6) ? 0xf0u : 0xe0u) >> w) & 1;
      asm volatile("s_nop 4" ::: "memory");
      dma4_if(d.pf, d.rp, d.vp + (lane >> 5) * d.pstep, 0, d.mp);
    }
#pragma unroll
    for (int i = 0; i < 2; ++i) d.vx[i] = (lane >> 3) * 1280 + (((lane & 7) ^ (lane >> 4) ^ (4 * i)) << 4);
    d.sx = (rt * 256 + w * 32) * 1280;
    d.mx = lds0 + w * 4096;
#pragma unroll
    for (int i = 0; i < 3; ++i) {
      const unsigned q = 64 * i + lane, kr = q / 12, pc = q % 12;
      int lc = (int)(pc ^ (((kr >> 3) & 1) << 1)) - (int)shift;
      lc = lc < 0 ? 0 : (lc > (int)cmax ? (int)cmax : lc);
      d.vw[i] = ((w & 3) * 16 + kr) * d.ldw + col0 * 2 + lc * 16;
    }
    d.mw = lds0 + L_W + half * WIMG + (w & 3) * 3072;
#pragma unroll
    for (int i = 0; i < 7; ++i) dma_piece(d, i, 0, 0);
    __builtin_amdgcn_s_barrier();
    dma4_if(d.pf, d.rp, d.vp, 2 * d.pstep, d.mp);
#pragma unroll
    for (int i = 0; i < 7; ++i) dma_piece(d, i, 1, 1);
    TAKE_TAG();

    const unsigned tg = w & 3, ch = w >> 2;
    const unsigned xa0 = lds0 + (tg * 64 + l15) * 128 + ((G ^ (l15 >> 1)) << 4), xa1 = xa0 ^ 64;
    const unsigned q4 = l15 >> 2, p4 = lane & 3;
    const unsigned wrow = lds0 + L_W + (8 * G + q4) * 192, wx = (G & 1) << 5;
    const unsigned rt64 = rt * 4 + tg;
    const unsigned tailpos = (lane >> 5) * 1024 + ((2 * ch + (G & 1)) * 16 + l15) * 16;

    if (opq(type) == 2) {
      unsigned wa[6];
#pragma unroll
      for (int i = 0; i < 3; ++i) { wa[i] = wrow + ((96 * ch + 32 * i + 8 * p4) ^ wx); wa[3 + i] = wa[i] + WIMG; asm volatile("" : "+v"(wa[3 + i])); }
      f32x4 acc[4][6];
#pragma unroll
      for (int i = 0; i < 24; ++i) acc[i / 6][i % 6] = f32x4{0.f, 0.f, 0.f, 0.f};
      const unsigned obase = WS_A + (rt64 * KS + 3 * ci) * 4096;
      u32x2 tl[2];
      auto epi = [&](int m) {
        unsigned o[6];
#pragma unroll
        for (int i = 0; i < 3; ++i) {
          const f32x4 hv = glu4(acc[m][i], acc[m][3 + i]);
          o[2 * i] = pk2(hv[0], hv[1]); o[2 * i + 1] = pk2(hv[2], hv[3]);
        }
        st128(u32x4{o[0], o[1], o[2], o[3]}, rws, obase + ch * 8192 + lane * 16 + m * 1024);
        tl[m & 1] = u32x2{o[4], o[5]};
        if (m & 1) st128(pair_tail(tl[0], tl[1]), rws, obase + 4096 + tailpos + (m - 1) * 1024);
      };
      gemm1_loop<6, true, 3, true>(acc, d, c, xa0, xa1, wa, epi STAMP_PASS);
      STAMP(5);
      DRAIN_BAR();
      flag_set(rws, WS_F1 + myflag, c.tag);
      preload_b(bq, fhv);
    }
    if (opq(type) == 0) {
      unsigned wa[5];
#pragma unroll
      for (int i = 0; i < 5; ++i) wa[i] = wrow + ch * WIMG + ((40 * p4 + 8 * i) ^ wx);
      f32x4 acc[4][5];
#pragma unroll
      for (int i = 0; i < 20; ++i) acc[i / 5][i % 5] = f32x4{PHI_EPS, PHI_EPS, PHI_EPS, PHI_EPS};
      auto epi = [&](int m) {
#pragma unroll
        for (int i = 0; i < 5; ++i) acc[m][i] = phi4(acc[m][i]);
        asm volatile("" : "+v"(acc[m][0]), "+v"(acc[m][1]), "+v"(acc[m][2]), "+v"(acc[m][3]), "+v"(acc[m][4]));
      };
      gemm1_loop<5, true, 4, true>(acc, d, c, xa0, xa1, wa, epi STAMP_PASS);
      const unsigned fgq = flag_addr<1>(lane, WS_FG + (2 * rt + (tg >> 1)) * FS);
      flag_wait<1>(flag_load(rws, fgq), rws, fgq, c.tag);
      float gq[4];
#pragma unroll
      for (int m = 0; m < 4; ++m) gq[m] = __builtin_bit_cast(float, __builtin_amdgcn_raw_buffer_load_b32(rws, WS_G + (rt64 * 64 + m * 16 + l15) * 4, 0, SC1));
      LDS_BAR();
      preload_b(bq, fhv);
      STAMP(5);
      if (w == 7) {
        const unsigned fkv = flag_addr<16>(lane, WS_F1 + ((lane & 7) * 32 + 8 * ci + 1 + (lane >> 3)) * FS);
        flag_wait<16>(flag_load(rws, fkv), rws, fkv, c.tag);
        STAMP(6);
        const unsigned zo = sel(lane < 40, WS_KVP + (8 * ci + lane / 5) * 1680 + 1600 + (lane % 5) * 16, OOB);
        f32x4 p[8];
#pragma unroll
        for (int r = 0; r < 8; ++r) p[r] = F4(ld128(rws, zo + r * (32 * 1680)));
        ((LAS f32x4*)(lds0 + L_SCR))[lane] = ((p[0] + p[1]) + (p[2] + p[3])) + ((p[4] + p[5]) + (p[6] + p[7]));
      }
      LDS_BAR();
      f32x4 z[5];
#pragma unroll
      for (int i = 0; i < 5; ++i) z[i] = ((const LAS f32x4*)(lds0 + L_SCR))[(4 * ch + G) * 5 + i];
      const unsigned obase = WS_A + (rt64 * KS + KSH + 5 * ci) * 4096;
      u32x2 tl[2];
#pragma unroll
      for (int m = 0; m < 4; ++m) {
        f32x4 dv = acc[m][0] * z[0];
#pragma unroll
        for (int i = 1; i < 5; ++i) dv += acc[m][i] * z[i];
        const float sc = gq[m] * __builtin_amdgcn_rcpf((dv[0] + dv[1]) + (dv[2] + dv[3]) + 1e-6f);
        unsigned o[10];
#pragma unroll
        for (int i = 0; i < 5; ++i) { const f32x4 v = acc[m][i] * sc; o[2 * i] = pk2(v[0], v[1]); o[2 * i + 1] = pk2(v[2], v[3]); }
        st128(u32x4{o[0], o[1], o[2], o[3]}, rws, obase + ch * 8192 + lane * 16 + m * 1024);
        st128(u32x4{o[4], o[5], o[6], o[7]}, rws, obase + ch * 8192 + 4096 + lane * 16 + m * 1024);
        tl[m & 1] = u32x2{o[8], o[9]};
        if (m & 1) st128(pair_tail(tl[0], tl[1]), rws, obase + 16384 + tailpos + (m - 1) * 1024);
      }
      STAMP(7);
      DRAIN_BAR();
      flag_set(rws, WS_F1 + myflag, c.tag);
    }
    if (opq(type) == 1) {
      unsigned wa[5];
      const unsigned cA = 80 * ch;
      wa[0] = wrow + ((cA + 8 * p4) ^ wx);
      wa[1] = wrow + WIMG + ((cA + 16 + 8 * p4) ^ wx);
      wa[2] = wrow + ((cA + 40 + 8 * p4) ^ wx);
      wa[3] = wrow + WIMG + ((cA + 56 + 8 * p4) ^ wx);
      wa[4] = wrow + (p4 & 1) * WIMG + ((cA + 32 + (p4 & 1) * 16 + (p4 >> 1) * 40) ^ wx);
      const bool isk4 = ((l15 >> 2) & 1) == 0;
      f32x4 acc[4][5];
#pragma unroll
      for (int i = 0; i < 20; ++i) {
        const float v0 = (i % 5 == 0 || i % 5 == 2) ? PHI_EPS : (i % 5 == 4 ? (isk4 ? PHI_EPS : 0.f) : 0.f);
        acc[i / 5][i % 5] = f32x4{v0, v0, v0, v0};
      }
      f32x4 pr[7];
#pragma unroll
      for (int i = 0; i < 7; ++i) pr[i] = f32x4{0.f, 0.f, 0.f, 0.f};
      u32x4 fb[5];
      auto epi = [&](int m) {
#pragma unroll
        for (int b = 0; b < 5; ++b) {
          f32x4 v = acc[m][b];
          if (b == 0 || b == 2) v = phi4(v);
          unsigned u0 = pk2(v[0], v[1]), u1 = pk2(v[2], v[3]);
          if (b == 4) { const f32x4 ph = phi4(v); u0 = sel(isk4, pk2(ph[0], ph[1]), u0); u1 = sel(isk4, pk2(ph[2], ph[3]), u1); }
          fb[b][2 * (m & 1)] = u0; fb[b][2 * (m & 1) + 1] = u1;
        }
        if (m & 1) {
          u32x4 f4y;
#pragma unroll
          for (int e = 0; e < 4; ++e) f4y[e] = isk4 ? 0x3f803f80u : fb[4][e];
          pr[0] = mfma16(BF(fb[0]), BF(fb[1]), pr[0]);
          pr[1] = mfma16(BF(fb[0]), BF(f4y), pr[1]);
          pr[2] = mfma16(BF(fb[4]), BF(fb[1]), pr[2]);
          pr[3] = mfma16(BF(fb[4]), BF(f4y), pr[3]);
          pr[4] = mfma16(BF(fb[2]), BF(fb[3]), pr[4]);
          pr[5] = mfma16(BF(fb[2]), BF(f4y), pr[5]);
          pr[6] = mfma16(BF(fb[4]), BF(fb[3]), pr[6]);
        } else {
          asm volatile("" : "+v"(fb[0]), "+v"(fb[1]), "+v"(fb[2]), "+v"(fb[3]), "+v"(fb[4]));
        }
      };
      gemm1_loop<5, false, 4, false>(acc, d, c, xa0, xa1, wa, epi STAMP_PASS);
      {
        const unsigned rA = lds0 + (tg * 4 + 2 * ch) * 1680, rB = rA + 1680, dump = lds0 + 32768 + lane * 16;
        const bool ca = (l15 == 0) | ((l15 >> 2) == 1), cb = (l15 == 0) | ((l15 >> 2) == 3);
        const unsigned e4 = (l15 == 0 ? 20 : 16 + (l15 & 3)) * 80;
        *(LAS f32x4*)(rA + l15 * 80 + 16 * G) = pr[0];
        *(LAS f32x4*)(rB + l15 * 80 + 16 * G) = pr[4];
        *(LAS f32x4*)sel(ca, rA + e4 + 16 * G, dump) = pr[1];
        *(LAS f32x4*)sel(cb, rB + e4 + 16 * G, dump) = pr[5];
        *(LAS f32x4*)sel(G == 0, rA + l15 * 80 + 64, dump) = pr[2];
        *(LAS f32x4*)sel(G == 2, rB + l15 * 80 + 64, dump) = pr[6];
        *(LAS f32x4*)sel((G == 0) & ca, rA + e4 + 64, sel((G == 2) & cb, rB + e4 + 64, dump)) = pr[3];
      }
      LDS_BAR();
      {
        const LAS f32x4* r4 = (const LAS f32x4*)(lds0) + tid;
        st128(U4((r4[0] + r4[420]) + (r4[840] + r4[1260])), rws, sel(tid < 420, WS_KVP + (rt * 32 + ci * 4) * 1680 + tid * 16, OOB));
      }
      STAMP(5);
      DRAIN_BAR();
      flag_set(rws, WS_F1 + myflag, c.tag);
#pragma unroll
      for (int i = 0; i < 15; ++i) dma_early(c, i);
      preload_b(bq, fhv);
    }
  }
  phase2(bq, fhv);

}

extern "C" void kernel_launch(void* const* d_in, const int* in_sizes, int n_in, void* d_out, int out_size, void* d_ws,
                              size_t ws_size, hipStream_t stream) {
  (void)hipFuncSetAttribute((const void*)fused_block, hipFuncAttributeMaxDynamicSharedMemorySize, LDS_BYTES);
  fused_block<<<256, 512, LDS_BYTES, stream>>>((const uint16_t*)d_in[0], (const uint16_t*)d_in[1], (const uint16_t*)d_in[2],
                                               (const uint16_t*)d_in[4], (const uint16_t*)d_in[5], (const uint16_t*)d_in[6],
                                               (const uint16_t*)d_in[7], (unsigned char*)d_ws, (uint16_t*)d_out);
}
```

```cpp
#include <hip/hip_runtime.h>
#include <stdint.h>
#include <type_traits>

typedef short bf16x8 __attribute__((ext_vector_type(8)));
typedef short s16x4 __attribute__((ext_vector_type(4)));
typedef float f32x4 __attribute__((ext_vector_type(4)));
typedef float f32x2 __attribute__((ext_vector_type(2)));
typedef __bf16 bf16x2_t __attribute__((ext_vector_type(2)));
typedef unsigned u32x4 __attribute__((ext_vector_type(4)));
typedef unsigned u32x2 __attribute__((ext_vector_type(2)));
#define LAS __attribute__((address_space(3)))
#define DI __device__ __forceinline__
typedef __amdgpu_buffer_rsrc_t rsrc_t;

constexpr int S_ = 2048, D_ = 640, F_ = 1728;
constexpr int KSH = 54, KS = 74;
constexpr int SC1 = 16;

constexpr unsigned WS_E = 0;
constexpr unsigned FS = 128;
constexpr unsigned WS_F1 = 1024;
constexpr unsigned WS_FM = WS_F1 + 256 * FS;
constexpr unsigned WS_FT = WS_FM + 16 * FS;
constexpr unsigned WS_A = 131072;
constexpr unsigned WS_A_BYTES = 32u * KS * 4096u;
constexpr unsigned WS_B = WS_A + WS_A_BYTES;
constexpr unsigned WS_B_BYTES = 8u * KS * 5120u;
constexpr unsigned WS_KVP = WS_B + WS_B_BYTES;
constexpr unsigned WS_KVP_BYTES = 8u * 32u * 420u * 4u;
constexpr unsigned WS_G = WS_KVP + WS_KVP_BYTES;
constexpr unsigned WS_END = WS_G + 2048u * 4u;

DI rsrc_t mkrsrc(const void* p, unsigned bytes) { return __builtin_amdgcn_make_buffer_rsrc((void*)p, (short)0, (int)bytes, 0x00020000); }
DI unsigned rfl(unsigned v) { return __builtin_amdgcn_readfirstlane(v); }
template <int N> DI unsigned wrap(unsigned t) { unsigned r; asm("s_sub_u32 %0, %1, %2\n\ts_min_u32 %0, %0, %1" : "=&s"(r) : "s"(t), "i"(N) : "scc"); return r; }
constexpr unsigned OOB = 0x80000000u;
DI unsigned sel(bool c, unsigned a, unsigned b) { return c ? a : b; }
DI unsigned opq(unsigned v) { asm volatile("" : "+s"(v)); return v; }
DI unsigned opql(unsigned v) { asm volatile("" : "+v"(v)); return v; }
DI unsigned lane_id() { unsigned l; asm volatile("v_mbcnt_lo_u32_b32 %0, -1, 0\n\tv_mbcnt_hi_u32_b32 %0, -1, %0" : "=v"(l)); return l; }
#define FRESH_LANE() const unsigned lane = lane_id(), G = lane >> 4, l15 = lane & 15, tid = lane + 64 * w; (void)G; (void)l15; (void)tid
DI unsigned pk2(float a, float b) { f32x2 v = {a, b}; return __builtin_bit_cast(unsigned, __builtin_convertvector(v, bf16x2_t)); }
DI float dot2(unsigned a, unsigned b, float c) { return __builtin_amdgcn_fdot2_f32_bf16(__builtin_bit_cast(bf16x2_t, a), __builtin_bit_cast(bf16x2_t, b), c, false); }
DI f32x4 mfma16(bf16x8 a, bf16x8 b, f32x4 c) { return __builtin_amdgcn_mfma_f32_16x16x32_bf16(a, b, c, 0, 0, 0); }
DI float sigmoidf_(float v) { return __builtin_amdgcn_rcpf(1.0f + __expf(-v)); }
constexpr float PHI_EPS = 1e-6f, LOG2E = 1.4426950408889634f;
DI f32x4 phi4(f32x4 u, float lo = PHI_EPS, float bias = -PHI_EPS * LOG2E) {
  const f32x4 a = u * LOG2E + bias;
  f32x4 e, m;
#pragma unroll
  for (int r = 0; r < 4; ++r) { e[r] = __builtin_amdgcn_fmed3f(__builtin_amdgcn_exp2f(a[r]), 0.f, 1.f); m[r] = __builtin_amdgcn_fmed3f(u[r], lo, 3.0e38f); }
  return e + m;
}
DI f32x4 glu4(f32x4 g, f32x4 u) {
  f32x4 e = g * -LOG2E;
#pragma unroll
  for (int r = 0; r < 4; ++r) e[r] = __builtin_amdgcn_exp2f(e[r]);
  e = e + 1.0f;
#pragma unroll
  for (int r = 0; r < 4; ++r) e[r] = __builtin_amdgcn_rcpf(e[r]);
  return e * u;
}
#define BF(v) __builtin_bit_cast(bf16x8, v)
#define F4(v) __builtin_bit_cast(f32x4, v)
#define U4(v) __builtin_bit_cast(u32x4, v)
DI u32x4 ld128(rsrc_t rs, unsigned v, unsigned s = 0) { return __builtin_amdgcn_raw_buffer_load_b128(rs, v, s, SC1); }
DI void st128(u32x4 d, rsrc_t rs, unsigned v) { __builtin_amdgcn_raw_buffer_store_b128(d, rs, v, 0, SC1); }
DI void st64(u32x2 d, rsrc_t rs, unsigned v) { __builtin_amdgcn_raw_buffer_store_b64(d, rs, v, 0, SC1); }

DI void dma16(rsrc_t rs, unsigned voff, unsigned soff, unsigned ldsb) {
  asm volatile("s_mov_b32 m0, %0\n\ts_nop 0\n\tbuffer_load_dwordx4 %1, %2, %3 offen lds" ::"s"(ldsb), "v"(voff), "s"(rs), "s"(soff) : "memory");
}
DI void dma16_sc1(rsrc_t rs, unsigned voff, unsigned soff, unsigned ldsb) {
  asm volatile("s_mov_b32 m0, %0\n\ts_nop 0\n\tbuffer_load_dwordx4 %1, %2, %3 offen sc1 lds" ::"s"(ldsb), "v"(voff), "s"(rs), "s"(soff) : "memory");
}
template <int OFF> DI void dma16_sc1_next(rsrc_t rs, unsigned voff, unsigned soff) {
  asm volatile("buffer_load_dwordx4 %0, %1, %2 offen offset:%3 sc1 lds" ::"v"(voff), "s"(rs), "s"(soff), "i"(OFF) : "memory");
}
DI void dma16_nt(rsrc_t rs, unsigned voff, unsigned soff, unsigned ldsb) {
  asm volatile("s_mov_b32 m0, %0\n\ts_nop 0\n\tbuffer_load_dwordx4 %1, %2, %3 offen nt lds" ::"s"(ldsb), "v"(voff), "s"(rs), "s"(soff) : "memory");
}
DI void dma16_sc1_if(unsigned on, rsrc_t rs, unsigned voff, unsigned soff, unsigned ldsb) {
  asm volatile("s_cmp_eq_u32 %4, 0\n\ts_cbranch_scc1 1f\n\ts_mov_b32 m0, %0\n\ts_nop 0\n\tbuffer_load_dwordx4 %1, %2, %3 offen sc1 lds\n1:"
               ::"s"(ldsb), "v"(voff), "s"(rs), "s"(soff), "s"(on) : "memory", "scc");
}
DI void dma16_if(unsigned on, rsrc_t rs, unsigned voff, unsigned soff, unsigned ldsb) {
  asm volatile("s_cmp_eq_u32 %4, 0\n\ts_cbranch_scc1 1f\n\ts_mov_b32 m0, %0\n\ts_nop 0\n\tbuffer_load_dwordx4 %1, %2, %3 offen lds\n1:"
               ::"s"(ldsb), "v"(voff), "s"(rs), "s"(soff), "s"(on) : "memory", "scc");
}
DI void dma4_if(unsigned on, rsrc_t rs, unsigned voff, unsigned soff, unsigned ldsb) {
  asm volatile("s_cmp_eq_u32 %4, 0\n\ts_cbranch_scc1 1f\n\ts_mov_b32 m0, %0\n\ts_nop 0\n\tbuffer_load_dword %1, %2, %3 offen lds\n1:"
               ::"s"(ldsb), "v"(voff), "s"(rs), "s"(soff), "s"(on) : "memory", "scc");
}
#define WAIT_BAR(N, N1, extra) asm volatile("s_cmp_lg_u32 %0, 0\n\ts_cbranch_scc1 1f\n\ts_waitcnt vmcnt(" #N ")\n1:\n\ts_waitcnt vmcnt(" #N1 ") lgkmcnt(0)\n\ts_barrier" ::"s"(extra) : "memory", "scc")
DI bf16x8 lds128(unsigned a) { return *(const LAS bf16x8*)(a); }
DI s16x4 ldstr(unsigned a) { return __builtin_amdgcn_ds_read_tr16_b64_v4i16((LAS s16x4*)(a)); }
#define SBAR() __builtin_amdgcn_sched_barrier(0)
#define DRAIN_BAR() asm volatile("s_waitcnt vmcnt(0) lgkmcnt(0)\n\ts_barrier" ::: "memory")
#define LDS_BAR() asm volatile("s_waitcnt lgkmcnt(0)\n\ts_barrier" ::: "memory")

template <int N> DI unsigned flag_addr(unsigned lane, unsigned off) { return sel(lane < N, off, OOB); }
DI u32x2 flag_load(rsrc_t rs, unsigned voff) { return __builtin_amdgcn_raw_buffer_load_b64(rs, voff, 0, SC1); }
template <int N, int SLEEP = 2> DI void flag_wait(u32x2 v, rsrc_t rs, unsigned voff, unsigned long long tag) {
  unsigned n = 1u << 16;
  const unsigned long long mask = N == 64 ? ~0ull : (1ull << (N & 63)) - 1;
  asm volatile("1:\n\tv_cmp_ne_u64 vcc, %3, %0\n\ts_and_b64 vcc, vcc, %5\n\ts_cbranch_scc0 2f\n\ts_sub_u32 %1, %1, 1\n\ts_cbranch_scc1 2f\n\ts_sleep %6\n\t"
               "buffer_load_dwordx2 %0, %2, %4, 0 offen sc1\n\ts_waitcnt vmcnt(0)\n\ts_branch 1b\n2:"
               : "+v"(v), "+s"(n) : "v"(voff), "s"(tag), "s"(rs), "s"(mask), "i"(SLEEP) : "memory", "scc", "vcc");
}

DI f32x4 gather_partials(rsrc_t rs, unsigned off) {
  f32x4 v[8];
#pragma unroll
  for (int r = 0; r < 8; ++r) v[r] = F4(ld128(rs, off, r * (32 * 1680)));
  return ((v[0] + v[1]) + (v[2] + v[3])) + ((v[4] + v[5]) + (v[6] + v[7]));
}

DI void flag_set_if(unsigned on, rsrc_t rs, unsigned off, unsigned long long tag) {
  unsigned long long t;
  asm volatile("s_cmp_eq_u32 %4, 0\n\ts_cbranch_scc1 1f\n\tv_mov_b64 %0, %3\n\ts_mov_b64 exec, 1\n\tbuffer_store_dwordx2 %0, off, %1, %2 sc1\n\ts_mov_b64 exec, -1\n1:" : "=&v"(t) : "s"(rs), "s"(off), "s"(tag), "s"(on) : "memory", "scc");
}

DI void publish(unsigned w, rsrc_t rs, unsigned off, unsigned long long tag) {
  __builtin_amdgcn_s_setprio(3);
  DRAIN_BAR();
  flag_set_if(1u >> w, rs, off, tag);
  __builtin_amdgcn_s_barrier();
}

constexpr unsigned XB = 32768, WB = 24576, WIMG = 12288;
constexpr unsigned L_TIL = 147456, L_SCR = 31 * 5120, LDS_BYTES = 163840, L_DUMP = LDS_BYTES - 256;

template <int NB> struct Frags { bf16x8 x[4]; s16x4 wl[NB], wh[NB]; };
struct Dma { rsrc_t rx, rw; unsigned vx[2], vw[3], sx, ldw, mx, mw; unsigned koff; };
DI unsigned ktile(const Dma& d, unsigned t) { return wrap<10>(t + d.koff); }
struct Ctx { rsrc_t rws; unsigned long long tag; unsigned vb, seven, hs0, slot0, ct, unit, w; const void* wd; };

DI void dma_x(const Dma& d, int i, unsigned t, unsigned xo, unsigned on) { dma16_if(on, d.rx, d.vx[i & 1], d.sx + i * (8 * 1280) + ktile(d, t) * 128, d.mx + xo + i * 1024); }
DI void dma_x(const Dma& d, int i, unsigned t, unsigned xo) { dma16(d.rx, d.vx[i & 1], d.sx + i * (8 * 1280) + ktile(d, t) * 128, d.mx + xo + i * 1024); }
DI void dma_w(const Dma& d, int i, unsigned t, unsigned wo, unsigned on) { dma16_if(on, d.rw, d.vw[i], ktile(d, t) * 64 * d.ldw, d.mw + wo + i * 1024); }
DI void dma_w(const Dma& d, int i, unsigned t, unsigned wo) { dma16(d.rw, d.vw[i], ktile(d, t) * 64 * d.ldw, d.mw + wo + i * 1024); }
DI void dma_b(const Ctx& c, int i) {
  const unsigned src = (c.hs0 + 3) * 5120 + i * 1024;
  if (i < 15 && i % 4 == 1) dma16_sc1_next<1024>(c.rws, c.vb, src - 1024);
  else if (i < 15 && i % 4 == 2) dma16_sc1_next<2048>(c.rws, c.vb, src - 2048);
  else if (i < 15 && i % 4 == 3) dma16_sc1_next<3072>(c.rws, c.vb, src - 3072);
  else if (i < 15) dma16_sc1(c.rws, c.vb, src, c.slot0 + i * 1024);
  else dma16_sc1_if(c.seven, c.rws, c.vb, src, c.slot0 + i * 1024);
}
DI unsigned tiled_flags(const Ctx& c) {
  const unsigned lane = lane_id();
  return sel(lane < 14, WS_FT + (5 * min(c.hs0 + (lane >> 1), 53u) + ((5 * c.ct + 4 * (lane & 1)) >> 3)) * FS, OOB);
}
DI unsigned frag_col(unsigned nbg, unsigned p) { return sel(nbg % 10 / 2 == 2, 16 * nbg + 4 * p, 32 * (nbg >> 1) + 8 * p + 4 * (nbg & 1)); }
DI void tile_load(rsrc_t rwd, unsigned cc, unsigned k, unsigned cg, unsigned w, unsigned lane, unsigned lds) {
  const unsigned r = lane >> 4;
  const unsigned f0 = 96 * cc + sel(k == 1, 48 * ((w >> 1) & 1) + 32 + 4 * ((w & 1) + 2 * (w >> 2)), 24 * k + 4 * w);
  dma16_nt(rwd, r * 1280 + (((lane & 15) ^ (((4 * w + r) & 7) << 1)) << 4), f0 * 1280 + cg * 256, lds + w * 1024);
}
DI void tile_store(rsrc_t rws, unsigned cc, unsigned k, unsigned cg, unsigned w, unsigned lane, unsigned lds) {
  const unsigned row = lane >> 2, nbg = 8 * cg + w;
  const unsigned a = lds + row * 256 + (((frag_col(nbg, lane & 3) * 2) & 255) ^ ((row & 7) << 5));
  st128(U4(__builtin_shufflevector(ldstr(a), ldstr(a + 4096), 0, 1, 2, 3, 4, 5, 6, 7)), rws, sel(cc < 18, WS_B + ((nbg / 5 * KS + 3 * cc + k) * 5 + nbg % 5) * 1024, OOB) + lane * 16);
}
template <int NB>
DI void read_piece(Frags<NB>& f, int r, unsigned xa, const unsigned (&wa)[NB], unsigned woff) {
  if (r == 0) f.x[0] = lds128(xa);
  else if (r <= 2 * NB) {
    const int b = (r - 1) >> 1;
    if ((r - 1) & 1) f.wh[b] = ldstr(wa[b] + woff + 768); else f.wl[b] = ldstr(wa[b] + woff);
  } else if (r < 2 * NB + 4) f.x[r - 2 * NB] = lds128(xa + (r - 2 * NB) * 2048);
}
template <int NB, bool SWAP, class Vm>
DI void half_step(f32x4 (&acc)[4][NB], const Frags<NB>& cur, Frags<NB>& nxt, unsigned xa, const unsigned (&wa)[NB], unsigned woff, Vm vm) {
#pragma unroll
  for (int g = 0; g < 8; ++g) {
    const int i0 = (g * 4 * NB) / 8, i1 = ((g + 1) * 4 * NB) / 8;
#pragma unroll
    for (int i = i0; i < i1; ++i) {
      const int m = i / NB, b = i % NB;
      const bf16x8 wf = __builtin_shufflevector(cur.wl[b], cur.wh[b], 0, 1, 2, 3, 4, 5, 6, 7);
      acc[m][b] = SWAP ? mfma16(wf, cur.x[m], acc[m][b]) : mfma16(cur.x[m], wf, acc[m][b]);
      asm volatile("" : "+v"(acc[m][b]));
      read_piece<NB>(nxt, i, xa, wa, woff);
      SBAR();
    }
    vm(g);
    SBAR();
  }
}

template <int NB, bool SWAP>
DI void mma_row(f32x4 (&acc)[4][NB], const Frags<NB>& f, int m) {
#pragma unroll
  for (int b = 0; b < NB; ++b) {
    const bf16x8 wf = __builtin_shufflevector(f.wl[b], f.wh[b], 0, 1, 2, 3, 4, 5, 6, 7);
    acc[m][b] = SWAP ? mfma16(wf, f.x[m], acc[m][b]) : mfma16(f.x[m], wf, acc[m][b]);
  }
}
template <int NMFMA, int NVALU> DI void interleave() {
#pragma unroll
  for (int i = 0; i < NMFMA; ++i) { __builtin_amdgcn_sched_group_barrier(0x8, 1, 0); __builtin_amdgcn_sched_group_barrier(0x402, NVALU, 0); }
}

template <int NB, bool SWAP, bool DEEPW, int NT, int NV, bool STAGE, int NST, bool PLAIN, class Epi>
DI void gemm1_loop(f32x4 (&acc)[4][NB], const Dma& d, const Ctx& c, unsigned xa0, const unsigned (&wa)[NB], Epi epi) {
  Frags<NB> f0, f1;
  __builtin_amdgcn_s_setprio(1);
  asm volatile("s_waitcnt vmcnt(%0)\n\ts_barrier" ::"i"(5) : "memory");
#pragma unroll
  for (int r = 0; r < 2 * NB + 4; ++r) read_piece<NB>(f0, r, xa0, wa, 0);
#pragma unroll
  for (int i = 2; i < 4; ++i) dma_x(d, i, 1, XB);
  SBAR();
  half_step<NB, SWAP>(acc, f0, f1, xa0 ^ 64, wa, 32 * 192, [&](int g) {
    if (g == 3) __builtin_amdgcn_s_setprio(0);
    if (g == 7) {
#pragma unroll
      for (int i = 0; i < 4; ++i) { if (!DEEPW) dma_x(d, i, 2, 2 * XB); else if (i < 3) dma_w(d, i, 2, 2 * WB); }
    }
  });
  unsigned ro = 0;
  auto two_tiles = [&](unsigned t2, auto last) __attribute__((always_inline)) {
    constexpr bool LAST = decltype(last)::value;
    u32x2 tf;
#pragma unroll
    for (int u = 0; u < 2; ++u) {
      const unsigned t = t2 + u, rn = wrap<3 * (DEEPW ? WB : XB)>(ro + (DEEPW ? WB : XB));
      const unsigned xa = DEEPW ? xa0 + (u ^ 1) * XB : xa0 + rn, xb = DEEPW ? (xa0 ^ 64) + (u ^ 1) * XB : xa ^ 64, wo = DEEPW ? 0 : (u ^ 1) * WB;
      unsigned wn[NB];
#pragma unroll
      for (int b = 0; b < NB; ++b) wn[b] = DEEPW ? wa[b] + rn : wa[b];
      __builtin_amdgcn_s_setprio(3);
      static_assert(NT == 0 || (NT == 1 && DEEPW), "");
      if (NT == 0 || LAST) asm volatile("s_waitcnt vmcnt(%0) lgkmcnt(0)\n\ts_barrier" ::"i"(DEEPW ? 3 : 4) : "memory");
      else if (u == 0) WAIT_BAR(0, 3, t2 ^ 4);
      else WAIT_BAR(3, 4, (5u >> t2) & 1);
      SBAR();
      if (u == 0 && NT && !LAST) flag_set_if(t2 == 4 ? 1u >> c.w : 0u, c.rws, WS_FT + c.unit * FS, c.tag);
      if (LAST && u == 0) { tf = flag_load(c.rws, tiled_flags(c)); SBAR(); }
      if (LAST && u == 1) flag_wait<14>(tf, c.rws, tiled_flags(c), c.tag);
      half_step<NB, SWAP>(acc, f1, f0, xa, wn, wo, [&](int g) {
        if (!(g & 1)) { if (DEEPW) dma_x(d, g >> 1, t + 2, u * XB); else if (g < 6) dma_w(d, g >> 1, t + 2, u * WB); }
        if (g == 7) __builtin_amdgcn_s_setprio(2);
      });
      half_step<NB, SWAP>(acc, f0, f1, xb, wn, wo + 32 * 192, [&](int g) {
        if (!(g & 1)) {
          if (!DEEPW) { if (u) dma_x(d, g >> 1, t + 3, ro, 6 - t2); else dma_x(d, g >> 1, t + 3, ro); }
          else if (g < 6) { if (u) dma_w(d, g >> 1, t + 3, ro, 6 - t2); else dma_w(d, g >> 1, t + 3, ro); }
        }
        if (LAST && u == 1) epi(g, -1);
        if (g == 3) __builtin_amdgcn_s_setprio(1);
        if (g == 6) __builtin_amdgcn_s_setprio(0);
      });
      ro = rn;
      if (NT && !LAST && u == 0 && t2 == 0) tile_load(mkrsrc(c.wd, F_ * D_ * 2), c.unit / 15, c.unit / 5 % 3, c.unit % 5, c.w, lane_id(), c.slot0 - c.w * 20480 + L_TIL);
      if (NT && !LAST && u == 0 && t2 == 2) tile_store(c.rws, c.unit / 15, c.unit / 5 % 3, c.unit % 5, c.w, lane_id(), c.slot0 - c.w * 20480 + L_TIL);
    }
  };
#pragma unroll 1
  for (unsigned t2 = 0; t2 < (PLAIN ? 6 : 8); t2 += 2) two_tiles(t2, std::false_type{});
  if constexpr (PLAIN) two_tiles(opq(6), std::true_type{});
  __builtin_amdgcn_s_setprio(3);
  if constexpr (PLAIN) {
    asm volatile("s_waitcnt vmcnt(8) lgkmcnt(0)\n\ts_barrier" ::: "memory");
    SBAR();
    half_step<NB, SWAP>(acc, f1, f0, xa0 + XB, wa, 0, [&](int g) { epi(8 + g, -1); if (g == 7) __builtin_amdgcn_s_setprio(2); });
    half_step<NB, SWAP>(acc, f0, f1, (xa0 ^ 64) + XB, wa, 32 * 192, [&](int g) {});
    LDS_BAR();
#pragma unroll
    for (int k = 0; k < 12; ++k) {
      if (k == 3) __builtin_amdgcn_s_setprio(1);
      if (k == 8) __builtin_amdgcn_s_setprio(0);
      if (k < 10) { dma_b(c, 2 * k); dma_b(c, 2 * k + 1); }
      SBAR();
      if (k == 0) mma_row<NB, SWAP>(acc, f1, 0);
      if (k % 3 == 0 && k < 9) mma_row<NB, SWAP>(acc, f1, k / 3 + 1);
      epi(k / 3, k % 3);
      interleave<NB, 6>();
      SBAR();
    }
    return;
  }
  DRAIN_BAR(); SBAR();
  u32x2 fv;
  if (STAGE) fv = flag_load(c.rws, tiled_flags(c));
  SBAR();
  Frags<NB>& fa = f0;
  Frags<NB> fb;
#pragma unroll
  for (int i = 0; i < 4 * NB; ++i) {
    const int b = i % NB, st = i / NB;
    const Frags<NB>& f = st < 2 ? f1 : (st == 2 ? fa : fb);
    const int m = st == 1 ? 1 : 0;
    const bf16x8 wf = __builtin_shufflevector(f.wl[b], f.wh[b], 0, 1, 2, 3, 4, 5, 6, 7);
    acc[m][b] = SWAP ? mfma16(wf, f.x[m], acc[m][b]) : mfma16(f.x[m], wf, acc[m][b]);
    asm volatile("" : "+v"(acc[m][b]));
    constexpr int NR = 2 * NB + 1;
#pragma unroll
    for (int r = 3 * i; r < 3 * i + 3; ++r) {
      constexpr unsigned X9 = DEEPW ? XB : 0, W9 = DEEPW ? 0 : WB;
      if (r < NR) read_piece<NB>(fa, r, xa0 + X9, wa, W9);
      else if (r < 2 * NR) read_piece<NB>(fb, r - NR, (xa0 ^ 64) + X9, wa, W9 + 32 * 192);
      else if (r < 2 * NR + 6) {
        if ((r - 2 * NR) & 1) read_piece<NB>(fb, NR + (r - 2 * NR) / 2, (xa0 ^ 64) + X9, wa, 0);
        else read_piece<NB>(fa, NR + (r - 2 * NR) / 2, xa0 + X9, wa, 0);
      }
    }
    SBAR();
    if (STAGE && i == 3 * NB - 1) {
      LDS_BAR();
      flag_wait<14>(fv, c.rws, tiled_flags(c), c.tag);
      SBAR();
    }
  }
#pragma unroll
  for (int k = 0; k < 9; ++k) {
    const int m = 1 + k / 3, part = k % 3;
    if (k == 3) __builtin_amdgcn_s_setprio(2);
    if (k == 6) __builtin_amdgcn_s_setprio(1);
#pragma unroll
    for (int i = (k * NST) / 9; i < ((k + 1) * NST) / 9; ++i) dma_b(c, i);
    SBAR();
    if (part == 0) mma_row<NB, SWAP>(acc, fa, m);
    if (part == 1) mma_row<NB, SWAP>(acc, fb, m);
    if (part == 2 && m < 3) mma_row<NB, SWAP>(acc, f1, m + 1);
    epi(m - 1, part);
    interleave<NB, NV>();
    SBAR();
  }
  __builtin_amdgcn_s_setprio(0);
  epi(3, 0); epi(3, 1); epi(3, 2);
}

DI u32x4 pair_tail(u32x2 a, u32x2 b) {
  const auto r0 = __builtin_amdgcn_permlane32_swap(a[0], b[0], false, false);
  const auto r1 = __builtin_amdgcn_permlane32_swap(a[1], b[1], false, false);
  return u32x4{r0[0], r1[0], r0[1], r1[1]};
}

__global__ void __launch_bounds__(512, 2)
fused_block(const uint16_t* x, const uint16_t* wqkv, const uint16_t* wo, const uint16_t* wg, const uint16_t* wu, const uint16_t* wd,
            const uint16_t* gw, unsigned char* ws, uint16_t* out) {
  extern __shared__ __attribute__((aligned(16))) unsigned char smem[];
  const unsigned lds0 = (unsigned)(uintptr_t)smem;
  const unsigned tid = threadIdx.x, lane = tid & 63, w = rfl(tid >> 6);
  const unsigned bid = blockIdx.x, xcd = bid & 7, idx = bid >> 3;
  const unsigned slot = idx >> 2, j = xcd >> 1, gn = 18 * j + idx - 12;
  const unsigned type = sel(idx < 4, 0, sel(idx < 12, 1, sel(idx < 30, 2, 3)));
  const unsigned rt = 4 * (xcd & 1) + sel(type < 2, idx & 3, sel(type == 2, gn & 3, j));
  Dma d;
  {
    unsigned long long pc; unsigned len;
    asm volatile("s_getpc_b64 %0\n.Lfb_code0:\n\ts_mov_b32 %1, .Lfb_code1-.Lfb_code0" : "=s"(pc), "=s"(len));
    dma4_if((0x0fffffffu >> idx) & (1u >> w), mkrsrc((const void*)pc, len), sel(lane < 16, (idx + 28 * lane) * 128, OOB), 0, lds0 + L_DUMP);
  }
  const unsigned G = lane >> 4, l15 = lane & 15;

  const unsigned ci = sel(type == 0, j, sel(type == 1, 2 * j + slot - 1, sel(type == 2, gn >> 2, idx - 30)));
  const unsigned l_w = lds0 + (type >= 2 ? 3 * XB : 2 * XB);
  {
    const unsigned gemm = type ^ 3;
    const bool glu = type >= 2;
    const unsigned half = w >> 2;
    const unsigned cstep = type == 0 ? 80 : 640, cbase = sel(type == 0, 160 * ci, 640 + 80 * ci);
    d.koff = (5 * ((ci + sel(type < 2, rt, 0)) & 1));
    d.ldw = glu ? F_ * 2 : 3 * D_ * 2;
#pragma unroll
    for (int i = 0; i < 2; ++i) d.vx[i] = opql((lane >> 3) * 1280 + (((lane & 7) ^ (lane >> 4) ^ (4 * i)) << 4));
    d.sx = opq((rt * 256 + w * 32) * 1280);
    d.mx = lds0 + w * 4096;
    const unsigned col0 = sel(glu, 96 * ci, cbase + cstep * half);
#pragma unroll
    for (int i = 0; i < 3; ++i) {
      const unsigned q = 64 * i + lane, kr = q / 12, pc = q % 12;
      d.vw[i] = opql(((w & 3) * 16 + kr) * d.ldw + ((col0 * 2) & ~63u) + ((pc ^ (((kr >> 3) & 1) << 1)) << 4));
    }
    d.mw = opq(l_w + half * WIMG + (w & 3) * 3072);
    asm volatile("" : "+s"(x), "+s"(wqkv), "+s"(wo), "+s"(wg), "+s"(wu), "+s"(wd), "+s"(gw), "+s"(ws), "+s"(out));
    d.rx = mkrsrc(x, S_ * D_ * 2);
    d.rw = mkrsrc(glu ? (half ? wu : wg) : wqkv, D_ * d.ldw);
#pragma unroll
    for (int i = 0; i < 4; ++i) dma_x(d, i, 0, 0, gemm);
#pragma unroll
    for (int i = 0; i < 3; ++i) dma_w(d, i, 0, 0, gemm);
    __builtin_amdgcn_s_barrier();
#pragma unroll
    for (int i = 0; i < 3; ++i) dma_w(d, i, 1, WB, gemm);
#pragma unroll
    for (int i = 0; i < 2; ++i) dma_x(d, i, 1, XB, gemm);
  }
  SBAR();

  const rsrc_t rws = mkrsrc(ws, WS_END);
  const unsigned myflag = (rt * 32 + ((0x1e0c0400u >> (8 * type)) & 31) + ci) * FS;
  const unsigned pan = 4 * (xcd & 1) + 2 * (j & 1) + (idx >> 4), sub = (idx >> 2) & 3, ct = 4 * (j >> 1) + (idx & 3), rt64p = 4 * pan + sub;
  Ctx c;
  c.rws = rws;
  c.hs0 = 7 * w - (w + 1) / 8;
  c.vb = WS_B + ct * KS * 5120 + lane * 16;
  c.seven = (0x3fu >> w) & 1;
  c.slot0 = lds0 + w * 20480;
  c.ct = ct; c.unit = 240 + bid;
  c.w = w; c.wd = wd;
  auto hstep = [&](int i) -> unsigned { return c.hs0 + i; };
  {
    unsigned long long epoch = *(const __attribute__((address_space(4))) unsigned long long*)(ws + WS_E);
    asm volatile("" : "+s"(epoch));
    c.tag = epoch + 1;
  }
  auto preload_b = [&](u32x4 (&bq)[3][5], int i0 = 0, int i1 = 15) __attribute__((always_inline)) {
#pragma unroll
    for (int i = i0; i < i1; ++i) bq[i / 5][i % 5] = ld128(rws, c.vb + (i % 5) * 1024, hstep(i / 5) * 5120);
    SBAR();
  };
  auto h_flags = [&]() {
    const unsigned l = lane_id();
    return flag_addr<4>(l, WS_F1 + (pan * 32 + sel(l < 3, 12 + min(c.hs0 / 3 + l, 17u), 30 + (sub >> 1))) * FS);
  };
  u32x2 fhv;
  u32x4 bq[3][5];
#define FRESH_BQ() _Pragma("unroll") for (int i_ = 0; i_ < 3; ++i_) asm volatile("" : "=v"(bq[i_][0]), "=v"(bq[i_][1]), "=v"(bq[i_][2]), "=v"(bq[i_][3]), "=v"(bq[i_][4]))

  {
    const unsigned tg = w & 3, ch = w >> 2;
    const unsigned xa0 = lds0 + (tg * 64 + l15) * 128 + ((G ^ (l15 >> 1)) << 4);
    const unsigned q4 = l15 >> 2, p4 = lane & 3;
    const unsigned wrow = l_w + (8 * G + q4) * 192, wx = (G & 1) << 5;
    const unsigned rt64 = rt * 4 + tg;
    const unsigned tailpos = (lane >> 5) * 1024 + ((2 * ch + (G & 1)) * 16 + l15) * 16;

    if (opq(type) == 2) {
      unsigned wa[6];
#pragma unroll
      for (int i = 0; i < 3; ++i) { wa[i] = wrow + ((96 * ch + 32 * i + 8 * p4) ^ wx); wa[3 + i] = wa[i] + WIMG; }
      f32x4 acc[4][6];
#pragma unroll
      for (int i = 0; i < 24; ++i) acc[i / 6][i % 6] = f32x4{0.f, 0.f, 0.f, 0.f};
      const unsigned obase = WS_A + (rt64 * KS + 3 * ci) * 4096;
      u32x2 tl[2];
      u32x2 lo;
      auto epi = [&](int m, int i) {
        const f32x4 hv = glu4(acc[m][i], acc[m][3 + i]);
        u32x2 o = {pk2(hv[0], hv[1]), pk2(hv[2], hv[3])};
        if (i != 1 && !(i == 2 && (m & 1))) asm volatile("" : "+v"(o));
        if (i == 0) lo = o;
        if (i == 1) st128(u32x4{lo[0], lo[1], o[0], o[1]}, rws, obase + ch * 8192 + lane * 16 + m * 1024);
        if (i == 2) tl[m & 1] = o;
        if (i == 2 && (m & 1)) st128(pair_tail(tl[0], tl[1]), rws, obase + 4096 + tailpos + (m - 1) * 1024);
      };
      gemm1_loop<6, true, false, 0, 3, true, 20, false>(acc, d, c, xa0, wa, epi);
      SBAR();
    }
    if (opq(type) == 1) {
      unsigned wa[5];
      const unsigned cA = 32 * (ci & 1) + 80 * ch;
      wa[0] = wrow + ((cA + 8 * p4) ^ wx);
      wa[1] = wa[0] + WIMG;
      wa[2] = wrow + ((cA + 40 + 8 * p4) ^ wx);
      wa[3] = wa[2] + WIMG;
      wa[4] = wrow + (p4 & 1) * WIMG + ((cA + 32 + (p4 >> 1) * 40) ^ wx);
      const bool isk4 = ((l15 >> 2) & 1) == 0;
      f32x4 acc[4][5];
#pragma unroll
      for (int i = 0; i < 20; ++i) {
        const float v0 = (i % 5 == 0 || i % 5 == 2) ? PHI_EPS : (i % 5 == 4 ? (isk4 ? PHI_EPS : 0.f) : 0.f);
        acc[i / 5][i % 5] = f32x4{v0, v0, v0, v0};
      }
      f32x4 pr[7];
#pragma unroll
      for (int i = 0; i < 7; ++i) pr[i] = f32x4{0.f, 0.f, 0.f, 0.f};
      u32x4 fb[5];
      u32x2 fv;
      auto epi = [&](int m, int part) {
        if (m == 2 && part == 0) fv = flag_load(rws, tiled_flags(c));
#pragma unroll
        for (int b = 2 * part; b < 2 * part + 2 && b < 5; ++b) {
          f32x4 v = acc[m][b];
          if (b == 0 || b == 2) v = phi4(v);
          if (b == 4) v = phi4(v, isk4 ? PHI_EPS : -3.0e38f, isk4 ? -PHI_EPS * LOG2E : -__builtin_inff());
          unsigned u0 = pk2(v[0], v[1]), u1 = pk2(v[2], v[3]);
          asm volatile("" : "+v"(u0), "+v"(u1));
          fb[b][2 * (m & 1)] = u0; fb[b][2 * (m & 1) + 1] = u1;
        }
        if ((m & 1) && part == 2) {
          u32x4 f4y;
#pragma unroll
          for (int e = 0; e < 4; ++e) f4y[e] = isk4 ? 0x3f803f80u : fb[4][e];
          pr[0] = mfma16(BF(fb[0]), BF(fb[1]), pr[0]);
          pr[1] = mfma16(BF(fb[0]), BF(f4y), pr[1]);
          pr[2] = mfma16(BF(fb[4]), BF(fb[1]), pr[2]);
          pr[3] = mfma16(BF(fb[4]), BF(f4y), pr[3]);
          pr[4] = mfma16(BF(fb[2]), BF(fb[3]), pr[4]);
          pr[5] = mfma16(BF(fb[2]), BF(f4y), pr[5]);
          pr[6] = mfma16(BF(fb[4]), BF(fb[3]), pr[6]);
        }
      };
      gemm1_loop<5, false, true, 0, 4, false, 0, false>(acc, d, c, xa0, wa, epi);
      FRESH_LANE();
      {
        const unsigned rA = lds0 + (tg * 4 + 2 * ch) * 1680, rB = rA + 1680, dump = lds0 + 30720 + lane * 16;
        const bool ca = (l15 == 0) | ((l15 >> 2) == 1), cb = (l15 == 0) | ((l15 >> 2) == 3);
        const unsigned e4 = (l15 == 0 ? 20 : 16 + (l15 & 3)) * 80;
        *(LAS f32x4*)(rA + l15 * 80 + 16 * G) = pr[0];
        *(LAS f32x4*)(rB + l15 * 80 + 16 * G) = pr[4];
        *(LAS f32x4*)sel(ca, rA + e4 + 16 * G, dump) = pr[1];
        *(LAS f32x4*)sel(cb, rB + e4 + 16 * G, dump) = pr[5];
        *(LAS f32x4*)sel(G == 0, rA + l15 * 80 + 64, dump) = pr[2];
        *(LAS f32x4*)sel(G == 2, rB + l15 * 80 + 64, dump) = pr[6];
        *(LAS f32x4*)sel((G == 0) & ca, rA + e4 + 64, sel((G == 2) & cb, rB + e4 + 64, dump)) = pr[3];
      }
      LDS_BAR();
      {
        const LAS f32x4* r4 = (const LAS f32x4*)(lds0) + tid;
        st128(U4((r4[0] + r4[420]) + (r4[840] + r4[1260])), rws, sel(tid < 420, WS_KVP + (rt * 32 + ci * 4) * 1680 + tid * 16, OOB));
      }
      publish(w, rws, WS_F1 + myflag, c.tag);
      flag_wait<14>(fv, rws, tiled_flags(c), c.tag);
#pragma unroll
      for (int i = 0; i < 20; ++i) dma_b(c, i);
    }
    FRESH_BQ();
    asm volatile("" : "=v"(fhv));
    if (opq(type) == 0) {
      unsigned wa[5];
#pragma unroll
      for (int i = 0; i < 5; ++i) wa[i] = wrow + ch * WIMG + ((32 * ch + 40 * p4 + 8 * i) ^ wx);
      f32x4 acc[4][5];
#pragma unroll
      for (int i = 0; i < 20; ++i) acc[i / 5][i % 5] = f32x4{PHI_EPS, PHI_EPS, PHI_EPS, PHI_EPS};
      auto epi = [&](int m, int part) {
        if (part < 0) { if (m < 15) preload_b(bq, m, m + 1); return; }
#pragma unroll
        for (int i = 2 * part; i < 2 * part + 2 && i < 5; ++i) { acc[m][i] = phi4(acc[m][i]); asm volatile("" : "+v"(acc[m][i])); }
      };
      gemm1_loop<5, true, true, 1, 4, true, 20, true>(acc, d, c, xa0, wa, epi);
      FRESH_LANE();
      const unsigned tailpos = (lane >> 5) * 1024 + ((2 * ch + (G & 1)) * 16 + l15) * 16;
      if (opq(w) == 7) {
        const unsigned fkv = flag_addr<18>(lane, WS_F1 + sel(lane < 16, (lane & 7) * 32 + 4 + 2 * ci + (lane >> 3), rt * 32 + 14 + lane) * FS);
        const unsigned zo = opql(sel(lane < 40, WS_KVP + (8 * ci + lane / 5) * 1680 + 1600 + (lane % 5) * 16, OOB));
        flag_wait<18, 1>(flag_load(rws, fkv), rws, fkv, c.tag);
        const u32x4 gv = ld128(rws, WS_G + rt * 1024 + lane * 16);
        ((LAS f32x4*)(lds0 + L_SCR))[lane] = gather_partials(rws, zo);
        ((LAS u32x4*)(lds0 + L_SCR + 1024))[lane] = gv;
      }
      LDS_BAR();
      const f32x4 gq = ((const LAS f32x4*)(lds0 + L_SCR + 1024))[tg * 16 + l15];
      f32x4 z[5];
#pragma unroll
      for (int i = 0; i < 5; ++i) z[i] = ((const LAS f32x4*)(lds0 + L_SCR))[(4 * ch + G) * 5 + i];
      const unsigned obase = WS_A + (rt64 * KS + KSH + 5 * ci) * 4096;
      u32x2 tl[2];
#pragma unroll
      for (int m = 0; m < 4; ++m) {
        f32x4 dv = acc[m][0] * z[0];
#pragma unroll
        for (int i = 1; i < 5; ++i) dv += acc[m][i] * z[i];
        const float sc = gq[m] * __builtin_amdgcn_rcpf((dv[0] + dv[1]) + (dv[2] + dv[3]) + 1e-6f);
        unsigned o[10];
#pragma unroll
        for (int i = 0; i < 5; ++i) { const f32x4 v = acc[m][i] * sc; o[2 * i] = pk2(v[0], v[1]); o[2 * i + 1] = pk2(v[2], v[3]); }
        st128(u32x4{o[0], o[1], o[2], o[3]}, rws, obase + ch * 8192 + lane * 16 + m * 1024);
        st128(u32x4{o[4], o[5], o[6], o[7]}, rws, obase + ch * 8192 + 4096 + lane * 16 + m * 1024);
        tl[m & 1] = u32x2{o[8], o[9]};
        if (m & 1) st128(pair_tail(tl[0], tl[1]), rws, obase + 16384 + tailpos + (m - 1) * 1024);
      }
      fhv = flag_load(rws, h_flags());
      publish(w, rws, WS_F1 + myflag, c.tag);
    }
  }
  if (opq(type) == 3) {
    FRESH_LANE();
    const unsigned sp = 2 * rt + ci, hA = 4 * (sp >> 1) + (sp & 1);
#pragma unroll
    for (int k = 0; k < 15; ++k) tile_load(mkrsrc(wd, F_ * D_ * 2), sp, k / 5, k % 5, w, lane, lds0 + k * 8192);
#pragma unroll
    for (int k = 0; k < 15; ++k) {
      if (k % 5 == 0) asm volatile("s_waitcnt vmcnt(10) lgkmcnt(0)\n\ts_barrier" ::: "memory");
      if (k == 10) {
#pragma unroll
        for (int i = 0; i < 7; ++i) {
          const unsigned q = min(7 * w + i, 49u);
          dma16(mkrsrc(wo, D_ * D_ * 2), lane * 16, 20 * hA * 1280 + q * 1024 + sel(q < 25, 0, 25600), lds0 + q * 1024);
        }
      }
      tile_store(rws, sp, k / 5, k % 5, w, lane, lds0 + k * 8192);
    }
    DRAIN_BAR();
    st64(u32x2{(unsigned)c.tag, (unsigned)(c.tag >> 32)}, rws, sel(tid < 15, WS_FT + (15 * sp + tid) * FS, OOB));
    const rsrc_t rx = mkrsrc(x, S_ * D_ * 2), rg = mkrsrc(gw, D_ * 2);
    const unsigned tok0 = sp * 128 + w * 16 + G;
    u32x4 gv[5], xv[4][5];
#pragma unroll
    for (int cc = 0; cc < 5; ++cc) gv[cc] = __builtin_amdgcn_raw_buffer_load_b128(rg, (cc * 16 + l15) * 16, 0, 0);
#pragma unroll
    for (int r = 0; r < 4; ++r)
#pragma unroll
      for (int cc = 0; cc < 5; ++cc) xv[r][cc] = __builtin_amdgcn_raw_buffer_load_b128(rx, (tok0 + r * 4) * 1280 + (cc * 16 + l15) * 16, 0, 0);
    const unsigned q4 = l15 >> 2, p4 = lane & 3;
    SBAR();
    ((LAS u32x2*)(lds0 + L_SCR))[tid] = u32x2{0, 0};
    const u32x2 fsv = flag_load(rws, tiled_flags(c));
    bf16x8 wof[2][5];
#pragma unroll
    for (int i = 0; i < 10; ++i) {
      const unsigned wb = lds0 + (i / 5) * 25600 + frag_col(5 * w + i % 5, p4) * 2;
      wof[i / 5][i % 5] = __builtin_shufflevector(ldstr(wb + min(8 * G + q4, 19u) * 1280), ldstr(wb + min(8 * G + 4 + q4, 19u) * 1280), 0, 1, 2, 3, 4, 5, 6, 7);
    }
    SBAR();
#pragma unroll
    for (int r = 0; r < 4; ++r) {
      float s = 0.f;
#pragma unroll
      for (int cc = 0; cc < 5; ++cc)
#pragma unroll
        for (int e = 0; e < 4; ++e) s = dot2(xv[r][cc][e], gv[cc][e], s);
      s += __shfl_xor(s, 1); s += __shfl_xor(s, 2); s += __shfl_xor(s, 4); s += __shfl_xor(s, 8);
      __builtin_amdgcn_raw_buffer_store_b32(__builtin_bit_cast(unsigned, sigmoidf_(s)), rws, sel(l15 == 0, WS_G + ((tok0 & ~63u) + (G + r * 4) * 4 + (w & 3)) * 4, OOB), 0, SC1);
    }
    publish(w, rws, WS_F1 + myflag, c.tag);
    flag_wait<14>(fsv, rws, tiled_flags(c), c.tag);
#pragma unroll
    for (int i = 0; i < 20; ++i) dma_b(c, i);
    preload_b(bq);
    if (w < 4) {
      const unsigned t = tid & 127;
      const unsigned fkv = flag_addr<8>(lane, WS_F1 + (lane * 32 + 4 + (sp >> 1)) * FS);
      const unsigned ko = opql(sel(t < 100, WS_KVP + (hA + 2 * (w >> 1)) * 1680 + t * 16, OOB)), kd = opql(lds0 + L_SCR + (w >> 1) * 2048 + ((t % 5) * 128 + t / 5) * 2);
      flag_wait<8, 1>(flag_load(rws, fkv), rws, fkv, c.tag);
      const f32x4 sv = gather_partials(rws, ko);
      const unsigned u0 = pk2(sv[0], sv[1]), u1 = pk2(sv[2], sv[3]);
      LAS unsigned short* ks = (LAS unsigned short*)kd;
      ks[0] = (unsigned short)u0; ks[32] = (unsigned short)(u0 >> 16); ks[64] = (unsigned short)u1; ks[96] = (unsigned short)(u1 >> 16);
    }
    LDS_BAR();
    {
      bf16x8 a0[2], a1[2];
#pragma unroll
      for (int k = 0; k < 2; ++k) { a0[k] = lds128(lds0 + L_SCR + k * 2048 + l15 * 64 + G * 16); a1[k] = lds128(lds0 + L_SCR + k * 2048 + (16 + (l15 & 3)) * 64 + G * 16); }
      const unsigned kch = (sp >> 1) & 1;
      const unsigned mo = WS_B + (w * KS + KSH + 5 * (sp >> 2)) * 5120 + l15 * 16;
#pragma unroll
      for (int i = 0; i < 5; ++i) {
        const f32x4 z4 = {0.f, 0.f, 0.f, 0.f};
        const f32x4 dA = mfma16(a0[0], wof[0][i], z4), dB = mfma16(a0[1], wof[1][i], z4), tA = mfma16(a1[0], wof[0][i], z4), tB = mfma16(a1[1], wof[1][i], z4);
        const auto r0 = __builtin_amdgcn_permlane16_swap(pk2(dA[0], dA[1]), pk2(dB[0], dB[1]), false, false);
        const auto r1 = __builtin_amdgcn_permlane16_swap(pk2(dA[2], dA[3]), pk2(dB[2], dB[3]), false, false);
        st128(u32x4{r0[0], r1[0], r0[1], r1[1]}, rws, mo + i * 1024 + (2 * kch + (G >> 1)) * 5120 + ((sp & 1) + 2 * (G & 1)) * 256);
        st128(u32x4{pk2(tA[0], tA[1]), pk2(tA[2], tA[3]), pk2(tB[0], tB[1]), pk2(tB[2], tB[3])}, rws, sel(G == 0, mo + i * 1024 + 4 * 5120 + (2 * kch + (sp & 1)) * 256, OOB));
      }
    }
    fhv = flag_load(rws, h_flags());
    publish(w, rws, WS_FM + sp * FS, c.tag);
  }
  if (opq(type) - 1 < 2) {
    if (opq(type) == 2) publish(w, rws, WS_F1 + myflag, c.tag);
    preload_b(bq);
    fhv = flag_load(rws, h_flags());
  }
  {
    FRESH_LANE();
    f32x4 acc[4][5];
#pragma unroll
    for (int i = 0; i < 20; ++i) acc[i / 5][i % 5] = f32x4{0.f, 0.f, 0.f, 0.f};
    const unsigned va = WS_A + rt64p * KS * 4096 + lane * 16;
    u32x4 aq[3][4];
    auto mma = [&](int ia, int ib) __attribute__((always_inline)) {
#pragma unroll
      for (int m = 0; m < 4; ++m)
#pragma unroll
        for (int b = 0; b < 5; ++b) { acc[m][b] = mfma16(BF(bq[ib][b]), BF(aq[ia][m]), acc[m][b]); asm volatile("" : "+v"(acc[m][b])); }
      SBAR();
    };
    __builtin_amdgcn_s_setprio(2);
    flag_wait<4>(fhv, rws, h_flags(), c.tag);
#pragma unroll
    for (int i = 0; i < 3; ++i) {
#pragma unroll
      for (int m = 0; m < 4; ++m) aq[i][m] = ld128(rws, va + m * 1024, hstep(i) * 4096);
      SBAR();
    }
    const f32x4 gm = F4(ld128(rws, WS_G + (rt64p * 64 + l15 * 4) * 4));
    SBAR();
    const unsigned fp = sel(lane < 4, WS_FM + ((4 * w + lane) & 15) * FS, sel(lane == 4, WS_F1 + (pan * 32 + (w & 3)) * FS, OOB));
    u32x2 fpv;
#pragma unroll
    for (int i = 0; i < 6; ++i) {
      mma(i % 3, i % 3);
      if (i < 3 || (i == 3 && opq(w) < 6)) {
#pragma unroll
        for (int m = 0; m < 4; ++m) aq[i % 3][m] = ld128(rws, va + m * 1024, hstep(i + 3) * 4096);
      }
      if (i < 4) {
        const unsigned sa = c.slot0 + i * 5120 + lane * 16;
#pragma unroll
        for (int b = 0; b < 5; ++b) bq[i % 3][b] = U4(lds128(sa + b * 1024));
      }
      if (i == 3) __builtin_amdgcn_s_setprio(1);
      if (i == 3) fpv = flag_load(rws, fp);
      SBAR();
    }
    auto scale = [&]() __attribute__((always_inline)) {
#pragma unroll
      for (int i = 0; i < 20; ++i) acc[i / 5][i % 5] *= 1.0f - gm[i / 5];
      SBAR();
    };
    auto park = [&](int m) __attribute__((always_inline)) {
#pragma unroll
      for (int b = 0; b < 5; ++b) *(LAS f32x4*)(c.slot0 + (m * 5 + b) * 1024 + lane * 16) = acc[m][b];
    };
    u32x2 fall;
    if (bid == 0) fall = flag_load(rws, sel(tid < 256, WS_F1 + tid * FS, WS_F1));
    if (opq(w) >= 4) {
      if (opq(w) < 6) mma(0, 0);
      scale();
#pragma unroll
      for (int m = 0; m < 4; ++m) park(m);
    }
    if (opq(w) < 4) {
      const unsigned vpa = va + (KSH + 5 * w) * 4096, vpb = c.vb + (KSH + 5 * w) * 5120;
      u32x4 pa[5][4], pb[5][5];
      auto ld = [&](int f0, int f1) __attribute__((always_inline)) {
#pragma unroll
        for (int f = f0; f < f1; ++f) {
          const int i = f / 9, k = f % 9;
          if (k < 5) pb[i][k] = ld128(rws, vpb + k * 1024, i * 5120); else pa[i][k - 5] = ld128(rws, vpa + (k - 5) * 1024, i * 4096);
        }
        SBAR();
      };
      auto pmma = [&](int i, int m) __attribute__((always_inline)) {
#pragma unroll
        for (int b = 0; b < 5; ++b) { acc[m][b] = mfma16(BF(pb[i][b]), BF(pa[i][m]), acc[m][b]); asm volatile("" : "+v"(acc[m][b])); }
      };
      __builtin_amdgcn_s_setprio(0);
      flag_wait<5>(fpv, rws, fp, c.tag);
      ld(0, 14);
      __builtin_amdgcn_s_setprio(3);
      mma(0, 0);
      ld(14, 20);
      scale();
      ld(20, 27);
#pragma unroll
      for (int i = 0; i < 4; ++i) {
#pragma unroll
        for (int m = 0; m < 4; ++m) pmma(i, m);
        SBAR();
        if (i == 0) ld(27, 36);
        if (i == 1) ld(36, 45);
      }
#pragma unroll
      for (int m = 0; m < 5; ++m) {
        if (m < 4) pmma(4, m);
        if (m > 0) park(m - 1);
        SBAR();
      }
    }
    LDS_BAR();
    const rsrc_t ro = mkrsrc(out, S_ * D_ * 2);
    auto total = [&](unsigned f) __attribute__((always_inline)) {
      f32x4 p[8];
#pragma unroll
      for (int ww = 0; ww < 8; ++ww) p[ww] = *(const LAS f32x4*)(lds0 + ww * 20480 + f * 1024 + lane * 16);
      const f32x4 v = ((p[0] + p[1]) + (p[2] + p[3])) + ((p[4] + p[5]) + (p[6] + p[7]));
      return u32x2{pk2(v[0], v[1]), pk2(v[2], v[3])};
    };
    {
      const unsigned b0 = 2 * (w & 1) + (ct & 1);
      const u32x2 lo = total((w >> 1) * 5 + b0), hi = total((w >> 1) * 5 + b0 + 1);
      __builtin_amdgcn_raw_buffer_store_b128(u32x4{lo[0], lo[1], hi[0], hi[1]}, ro, (rt64p * 64 + (w >> 1) * 16 + l15) * 1280 + (ct * 80 + b0 * 16 + 8 * G) * 2, 0, SC1);
    }
    if (w < 4) {
      const unsigned b1 = (ct & 1) ? 0 : 4;
      __builtin_amdgcn_raw_buffer_store_b64(total(w * 5 + b1), ro, (rt64p * 64 + w * 16 + l15) * 1280 + (ct * 80 + b1 * 16 + 4 * G) * 2, 0, SC1);
    }
    if (bid == 0) {
      flag_wait<64>(fall, rws, sel(tid < 256, WS_F1 + tid * FS, WS_F1), c.tag);
      __builtin_amdgcn_s_barrier();
      flag_set_if(1u >> w, rws, WS_E, c.tag);
    }
  }
  asm volatile(".Lfb_code1:");
}

extern "C" void kernel_launch(void* const* d_in, const int* in_sizes, int n_in, void* d_out, int out_size, void* d_ws,
                              size_t ws_size, hipStream_t stream) {
  (void)hipFuncSetAttribute((const void*)fused_block, hipFuncAttributeMaxDynamicSharedMemorySize, LDS_BYTES);
  fused_block<<<256, 512, LDS_BYTES, stream>>>((const uint16_t*)d_in[0], (const uint16_t*)d_in[1], (const uint16_t*)d_in[2],
                                               (const uint16_t*)d_in[4], (const uint16_t*)d_in[5], (const uint16_t*)d_in[6],
                                               (const uint16_t*)d_in[7], (unsigned char*)d_ws, (uint16_t*)d_out);
}
```
